```python
import torch

def _softmax(x, axis=-1):
    return torch.softmax(x, dim=axis)
def _gelu(x):
    return torch.nn.functional.gelu(x, approximate="tanh")
def _silu(x):
    return torch.nn.functional.silu(x)
def _sigmoid(x):
    return torch.sigmoid(x)
def _layernorm(x, axis=-1, eps=1e-5):
    m = x.mean(dim=axis, keepdim=True)
    v = x.var(dim=axis, keepdim=True, unbiased=False)
    return (x - m) / torch.sqrt(v + eps)
def _scatter_add(tgt, idx, src, axis=0):
    return tgt.clone().index_add_(axis, idx, src)
def _segment_sum(src, seg, n):
    out = torch.zeros((n,) + src.shape[1:], dtype=src.dtype)
    return out.index_add_(0, seg, src)
def _pad(x, pw):
    flat = tuple(v for pair in reversed(pw) for v in pair)
    return torch.nn.functional.pad(x, flat)

kernel_name = 'sequential_preLN__spectral_gfnet__geglu_ffn__prompt'

def setup_inputs():

    _w = (lambda a: (lambda t: t.to(torch.bfloat16) if t.is_floating_point() else t)(torch.as_tensor(a)))
    def _A_setup():
        B, S, D = 1, 2048, 640
        Df = D // 2 + 1
        return {
            "x": _w((lambda *s: torch.randn(*s, dtype=torch.bfloat16) * 0.05)(B, S, D)),
            "w_r": _w((lambda *s: torch.randn(*s, dtype=torch.bfloat16) * 0.4)(S, Df) * 0.02),
            "w_i": _w((lambda *s: torch.randn(*s, dtype=torch.bfloat16) * 0.4)(S, Df) * 0.02),
        }
    _A_in = _A_setup()

    def _B_setup():
        B, S, D, F = 1, 2048, 640, 1728
        return {
            "x": _w((lambda *s: torch.randn(*s, dtype=torch.bfloat16) * 0.05)(B, S, D)),
            "w_gate": _w((lambda *s: torch.randn(*s, dtype=torch.bfloat16) * 0.05)(D, F)),
            "w_up": _w((lambda *s: torch.randn(*s, dtype=torch.bfloat16) * 0.05)(D, F)),
            "w_down": _w((lambda *s: torch.randn(*s, dtype=torch.bfloat16) * 0.05)(F, D)),
        }
    _B_in = _B_setup()

    return {
        "A_x": _A_in["x"],
        "A_w_r": _A_in["w_r"],
        "A_w_i": _A_in["w_i"],
        "B_x": _B_in["x"],
        "B_w_gate": _B_in["w_gate"],
        "B_w_up": _B_in["w_up"],
        "B_w_down": _B_in["w_down"],
    }

def reference(A_x, A_w_r, A_w_i, B_x, B_w_gate, B_w_up, B_w_down):
    def _A_ref(x, w_r, w_i):
        B, S, D = x.shape
        X = (lambda x, axes=(-2,-1): torch.fft.rfft2(x.float(), dim=axes))(x, axes=(-2, -1))
        W = w_r + 1j * w_i
        X = X * W
        y = (lambda x, s, axes=(-2,-1): torch.fft.irfft2(x, s=s, dim=axes).to(torch.bfloat16))(X, (S, D), axes=(-2, -1))
        return y

    def _B_ref(x, w_gate, w_up, w_down):
        return (_gelu(x @ w_gate) * (x @ w_up)) @ w_down

    x = A_x
    h = x + _A_ref(_layernorm(x, axis=-1), A_w_r, A_w_i)
    h2 = h + _B_ref(_layernorm(h, axis=-1), B_w_gate, B_w_up, B_w_down)
    return h2
```

```cpp
#include <hip/hip_runtime.h>
#include <hip/hip_bf16.h>
#include <stdint.h>

#define DI __device__ __forceinline__
typedef unsigned short u16;
typedef uint32_t u32;
typedef _Float16 f16;
typedef f16 f16x2 __attribute__((ext_vector_type(2)));
typedef f16 f16x4 __attribute__((ext_vector_type(4)));
typedef f16 f16x8 __attribute__((ext_vector_type(8)));
typedef __bf16 bf16x2 __attribute__((ext_vector_type(2)));
typedef __bf16 bf16x8 __attribute__((ext_vector_type(8)));
typedef short s16x4 __attribute__((ext_vector_type(4)));
typedef float f32x2 __attribute__((ext_vector_type(2)));
typedef float f32x4 __attribute__((ext_vector_type(4)));
typedef float f32x16 __attribute__((ext_vector_type(16)));
typedef u32 u32x2 __attribute__((ext_vector_type(2)));
typedef u32 u32x4 __attribute__((ext_vector_type(4)));
typedef int i32x4 __attribute__((ext_vector_type(4)));
#define LAS __attribute__((address_space(3)))

constexpr size_t X1_OFF = 0;
constexpr size_t X1_SZ = 256ull * 648 * 16;
constexpr size_t WT_OFF = X1_OFF + X1_SZ;
constexpr size_t WT_SZ = 323ull * 8192;
constexpr size_t X2_OFF = WT_OFF + WT_SZ;
constexpr size_t X2_SZ = 160ull * 16384;
constexpr size_t EC_OFF = X2_OFF + X2_SZ;
constexpr size_t EC_SZ = 4096;
constexpr size_t H_OFF = EC_OFF + EC_SZ;
constexpr size_t H_SZ = 2048ull * 640 * 2;
constexpr size_t ACT_OFF = H_OFF + H_SZ;
constexpr size_t ACT_SZ = 64ull * 40 * 1024;
constexpr size_t W1_OFF = ACT_OFF + ACT_SZ;
constexpr size_t W1_SZ = 16ull * 280 * 1024;
constexpr size_t W2_OFF = W1_OFF + W1_SZ;
constexpr size_t W2_SZ = 8ull * 56 * 5 * 1024;
constexpr size_t G_OFF = W2_OFF + W2_SZ;
constexpr size_t G_SZ = 32ull * 56 * 4 * 1024;

constexpr int JT = 4;

DI float asf(u32 u) { return __builtin_bit_cast(float, u); }
DI u32 asu(float f) { return __builtin_bit_cast(u32, f); }
DI u32 pk_bf16(float a, float b) { f32x2 v = {a, b}; return __builtin_bit_cast(u32, __builtin_convertvector(v, bf16x2)); }
DI u32 pk_f16(float a, float b) { f32x2 v = {a, b}; return __builtin_bit_cast(u32, __builtin_convertvector(v, f16x2)); }
DI float bf_lo(u32 u) { return asf(u << 16); }
DI float bf_hi(u32 u) { return asf(u & 0xffff0000u); }
DI float bfr(float a) { return bf_lo(pk_bf16(a, a)); }
DI float cosr(float rev) { return __builtin_amdgcn_cosf(rev); }
DI float sinr(float rev) { return __builtin_amdgcn_sinf(rev); }
DI constexpr int crow(int reg, int h) { return (reg & 3) + 8 * (reg >> 2) + 4 * h; }

template <int CTRL> DI float dpp(float v) {
  return __builtin_bit_cast(float, __builtin_amdgcn_update_dpp(0, __builtin_bit_cast(int, v), CTRL, 0xf, 0xf, true));
}
DI float sum32(float v) {
  v += dpp<0xB1>(v); v += dpp<0x4E>(v); v += dpp<0x141>(v); v += dpp<0x140>(v);
  auto r = __builtin_amdgcn_permlane16_swap(asu(v), asu(v), false, false);
  return asf(r[0]) + asf(r[1]);
}
DI float sum64(float v) {
  v = sum32(v);
  auto r = __builtin_amdgcn_permlane32_swap(asu(v), asu(v), false, false);
  return asf(r[0]) + asf(r[1]);
}
DI float max64(float v) {
  v = fmaxf(v, dpp<0xB1>(v)); v = fmaxf(v, dpp<0x4E>(v)); v = fmaxf(v, dpp<0x141>(v)); v = fmaxf(v, dpp<0x140>(v));
  auto r = __builtin_amdgcn_permlane16_swap(asu(v), asu(v), false, false);
  v = fmaxf(asf(r[0]), asf(r[1]));
  auto s = __builtin_amdgcn_permlane32_swap(asu(v), asu(v), false, false);
  return fmaxf(asf(s[0]), asf(s[1]));
}
DI f32x16 mf16(f16x8 a, f16x8 b, f32x16 c) { return __builtin_amdgcn_mfma_f32_32x32x16_f16(a, b, c, 0, 0, 0); }
DI f32x16 mb32(u32x4 a, u32x4 b, f32x16 c) {
  return __builtin_amdgcn_mfma_f32_32x32x16_bf16(__builtin_bit_cast(bf16x8, a), __builtin_bit_cast(bf16x8, b), c, 0, 0, 0);
}
DI f32x4 mb16(u32x4 a, u32x4 b, f32x4 c) {
  return __builtin_amdgcn_mfma_f32_16x16x32_bf16(__builtin_bit_cast(bf16x8, a), __builtin_bit_cast(bf16x8, b), c, 0, 0, 0);
}
DI f16x8 asf16x8(u32x4 v) { return __builtin_bit_cast(f16x8, v); }
DI u32x2 tr_read(const char* p) {
  s16x4 r = __builtin_amdgcn_ds_read_tr16_b64_v4i16((LAS s16x4*)(p));
  return __builtin_bit_cast(u32x2, r);
}
DI f16x8 pack8(const f32x16& x, int s) {
  u32x4 p = {pk_f16(x[8 * s], x[8 * s + 1]), pk_f16(x[8 * s + 2], x[8 * s + 3]), pk_f16(x[8 * s + 4], x[8 * s + 5]),
             pk_f16(x[8 * s + 6], x[8 * s + 7])};
  return asf16x8(p);
}
DI f32x16 zero16() { f32x16 z; for (int i = 0; i < 16; ++i) z[i] = 0.f; return z; }
DI int wave_id() { return __builtin_amdgcn_readfirstlane(threadIdx.x >> 6); }

DI void ln_scalars(float mean, float var, float& mb, float& inv) {
  mb = bfr(mean);
  float vb = bfr(var);
  float t = bfr(vb + 1e-5f);
  float den = bfr(__builtin_amdgcn_sqrtf(t));
  inv = __builtin_amdgcn_rcpf(den);
}

constexpr int K1_IMG = 1344;
constexpr int K1_PLANE = 33 * 20 * 4;
constexpr int K1_LDS = 8 * K1_IMG + 4 * K1_PLANE;
constexpr int K1_WT_ITEMS = 41 * 64;
constexpr int K1_WT_PER_WAVE = 4;
constexpr int K1_WT_WGS = K1_WT_ITEMS / (4 * K1_WT_PER_WAVE);
constexpr int K1_GRID = 256 + K1_WT_WGS + 1;

DI void k1_row(const u16* __restrict__ x, char* __restrict__ X1, char* smem, int wg) {
  const int tid = threadIdx.x, lane = tid & 63, w = wave_id(), r = lane & 31, h = lane >> 5;
  const int b2 = wg >> 3, aq = wg & 7;
  const int m = 32 * (4 * aq + w) + b2;
  const u32x2* src = (const u32x2*)(x + (size_t)(2 * m + h) * 640) + r;
  u32x2 v[5];
#pragma unroll
  for (int i = 0; i < 5; ++i) v[i] = src[32 * i];

  f16x8 C1[2], S1[2], N1[2];
#pragma unroll
  for (int s = 0; s < 2; ++s)
#pragma unroll
    for (int j = 0; j < 8; ++j) {
      float rev = (float)(((16 * s + 8 * h + j) * r) & 31) * (1.f / 32.f);
      float c = cosr(rev), sn = sinr(rev);
      C1[s][j] = (f16)c; S1[s][j] = (f16)sn; N1[s][j] = (f16)(-sn);
    }
  float tc[12], ts[12];
#pragma unroll
  for (int i = 0; i < 12; ++i) {
    float rev = (float)(crow(i, 0) * r + 4 * h * r) * (1.f / 640.f);
    tc[i] = cosr(rev) * 0.125f; ts[i] = sinr(rev) * 0.125f;
  }
  f16x8 C2, S2, N2, MR, MI;
#pragma unroll
  for (int j = 0; j < 8; ++j) {
    float rev = (float)((crow(j, 0) + 4 * h) * r) * 0.05f;
    float c = cosr(rev), sn = sinr(rev);
    C2[j] = (f16)c; S2[j] = (f16)sn; N2[j] = (f16)(-sn);
  }
#pragma unroll
  for (int j = 0; j < 4; ++j) {
    float rev = (float)((16 + j) * r) * 0.05f;
    float c = h ? 0.f : cosr(rev), sn = h ? 0.f : sinr(rev);
    MR[j] = (f16)c; MR[4 + j] = (f16)sn; MI[j] = (f16)(-sn); MI[4 + j] = (f16)c;
  }

  float f[20];
#pragma unroll
  for (int i = 0; i < 5; ++i) {
    f[4 * i] = bf_lo(v[i][0]); f[4 * i + 1] = bf_hi(v[i][0]); f[4 * i + 2] = bf_lo(v[i][1]); f[4 * i + 3] = bf_hi(v[i][1]);
  }
  float s = 0.f;
#pragma unroll
  for (int i = 0; i < 20; ++i) s += f[i];
  const float mean = sum32(s) * (1.f / 640.f);
  float q2 = 0.f;
#pragma unroll
  for (int i = 0; i < 20; ++i) { float d = f[i] - mean; q2 += d * d; }
  const float var = sum32(q2) * (1.f / 640.f);
  float mb, inv;
  ln_scalars(mean, var, mb, inv);
  char* img = smem + w * (2 * K1_IMG);
#pragma unroll
  for (int i = 0; i < 5; ++i) {
    u32x2 o;
#pragma unroll
    for (int k = 0; k < 2; ++k) {
      u32 d = pk_bf16(f[4 * i + 2 * k] - mb, f[4 * i + 2 * k + 1] - mb);
      u32 n = pk_bf16(bf_lo(d) * inv, bf_hi(d) * inv);
      o[k] = pk_f16(bf_lo(n), bf_hi(n));
    }
    *(u32x2*)(img + h * K1_IMG + 8 * (r + 32 * i)) = o;
  }
  if (r < 8) { u32x2 z = {0u, 0u}; *(u32x2*)(img + h * K1_IMG + 1280 + 8 * r) = z; }
  __builtin_amdgcn_fence(__ATOMIC_RELEASE, "wavefront");
  __builtin_amdgcn_wave_barrier();
  __builtin_amdgcn_fence(__ATOMIC_ACQUIRE, "wavefront");

  const int q4 = (lane & 15) >> 2, p4 = lane & 3, blk = (lane >> 4) & 1;
  const char* tb = img + (8 * h + q4) * 40 + 32 * blk + 8 * p4;
  f32x16 Dr = zero16(), Di = zero16();
#pragma unroll
  for (int s2 = 0; s2 < 2; ++s2) {
    u32x2 r0 = tr_read(tb + 640 * s2), r1 = tr_read(tb + 640 * s2 + 160);
    u32x2 i0 = tr_read(tb + K1_IMG + 640 * s2), i1 = tr_read(tb + K1_IMG + 640 * s2 + 160);
    u32x4 ar = {r0[0], r0[1], r1[0], r1[1]}, ai = {i0[0], i0[1], i1[0], i1[1]};
    Dr = mf16(asf16x8(ar), C1[s2], Dr); Di = mf16(asf16x8(ai), C1[s2], Di);
    Dr = mf16(asf16x8(ai), S1[s2], Dr); Di = mf16(asf16x8(ar), N1[s2], Di);
  }
  float er[12], ei[12];
#pragma unroll
  for (int i = 0; i < 12; ++i) {
    er[i] = Dr[i] * tc[i] + Di[i] * ts[i];
    ei[i] = Di[i] * tc[i] - Dr[i] * ts[i];
  }
  u32x4 pr = {pk_f16(er[0], er[1]), pk_f16(er[2], er[3]), pk_f16(er[4], er[5]), pk_f16(er[6], er[7])};
  u32x4 pi = {pk_f16(ei[0], ei[1]), pk_f16(ei[2], ei[3]), pk_f16(ei[4], ei[5]), pk_f16(ei[6], ei[7])};
  u32x4 pm = {pk_f16(er[8], er[9]), pk_f16(er[10], er[11]), pk_f16(ei[8], ei[9]), pk_f16(ei[10], ei[11])};
  f32x16 Xr = zero16(), Xi = zero16();
  Xr = mf16(C2, asf16x8(pr), Xr); Xi = mf16(N2, asf16x8(pr), Xi);
  Xr = mf16(S2, asf16x8(pi), Xr); Xi = mf16(C2, asf16x8(pi), Xi);
  Xr = mf16(MR, asf16x8(pm), Xr); Xi = mf16(MI, asf16x8(pm), Xi);

  char* planes = smem + 8 * K1_IMG;
  u32* pl = (u32*)(planes + w * K1_PLANE) + 33 * 4 * h + r;
#pragma unroll
  for (int i = 0; i < 8; ++i) pl[33 * crow(i, 0)] = pk_f16(Xr[i], Xi[i]);
  if (h == 0) {
#pragma unroll
    for (int i = 8; i < 12; ++i) pl[33 * crow(i, 0)] = pk_f16(Xr[i], Xi[i]);
  }
  __syncthreads();
  u32x4* dst = (u32x4*)(X1 + (size_t)wg * (648 * 16));
#pragma unroll
  for (int it = 0; it < 3; ++it) {
    const int slot = tid + 256 * it;
    if (slot < 648) {
      const int g = slot >> 3, pos = slot & 7, pp = pos & 3, mir = pos >> 2;
      int a, b;
      if (g < 75) { int a1 = g / 5 + 1, c = g - 5 * (g / 5); a = mir ? 32 - a1 : a1; b = mir ? 19 - 4 * c - pp : 4 * c + pp; }
      else if (g < 78) { int c = g - 75; a = 16; b = mir ? 19 - 4 * c - pp : 4 * c + pp; }
      else { int c = g - 78, bb = 4 * c + pp; a = 0; b = mir ? (bb == 0 ? 0 : 20 - bb) : bb; }
      const u32* sp = (const u32*)planes + 33 * b + a;
      u32x4 o = {sp[0], sp[K1_PLANE / 4], sp[2 * (K1_PLANE / 4)], sp[3 * (K1_PLANE / 4)]};
      dst[slot] = o;
    }
  }
}

DI void k1_wt(const u16* __restrict__ wr, const u16* __restrict__ wi, char* __restrict__ WT, int wg) {
  const int tid = threadIdx.x, lane = tid & 63, w = wave_id();
  if (wg < K1_WT_WGS) {
    const int alo = lane & 7, qlo = lane >> 3;
#pragma unroll
    for (int it = 0; it < K1_WT_PER_WAVE; ++it) {
      const int item = (wg * 4 + w) * K1_WT_PER_WAVE + it;
      const int qo = item >> 6, i = (item >> 3) & 7, h = (item >> 2) & 1, ao = item & 3;
      const int q = 8 * qo + qlo, a = 8 * ao + alo;
      if (q <= 320) {
        const int p = a + 32 * (crow(2 * i, 0) + 4 * h);
        const int o00 = p * 321 + q, o01 = o00 + 1024 * 321, o10 = o00 + 32 * 321, o11 = o10 + 1024 * 321;
        u32x4 o = {(u32)wr[o00] | ((u32)wi[o00] << 16), (u32)wr[o01] | ((u32)wi[o01] << 16),
                   (u32)wr[o10] | ((u32)wi[o10] << 16), (u32)wr[o11] | ((u32)wi[o11] << 16)};
        *(u32x4*)(WT + (size_t)q * 8192 + i * 1024 + (32 * h + a) * 16) = o;
      }
    }
  } else {
#pragma unroll
    for (int it = 0; it < 4; ++it) {
      const int idx = tid + 256 * it;
      const int T = idx >> 9, i = (idx >> 6) & 7, l = idx & 63, a = l & 31, h = l >> 5;
      const int q = T ? 320 : 0;
      const int p = a + 32 * (crow(2 * i, 0) + 4 * h), p2 = p + 32;
      const int o00 = ((2048 - p) & 2047) * 321 + q, o01 = (1024 - p) * 321 + q;
      const int o10 = (2048 - p2) * 321 + q, o11 = (1024 - p2) * 321 + q;
      u32x4 o = {(u32)wr[o00] | ((u32)wi[o00] << 16), (u32)wr[o01] | ((u32)wi[o01] << 16),
                 (u32)wr[o10] | ((u32)wi[o10] << 16), (u32)wr[o11] | ((u32)wi[o11] << 16)};
      *(u32x4*)(WT + (size_t)(321 + T) * 8192 + i * 1024 + l * 16) = o;
    }
  }
}

__global__ __launch_bounds__(256) void k1_kernel(const u16* __restrict__ x, const u16* __restrict__ wr,
                                                 const u16* __restrict__ wi, char* __restrict__ ws) {
  __shared__ __attribute__((aligned(16))) char smem[K1_LDS];
  const int bid = blockIdx.x;
  if (bid < 256) k1_row(x, ws + X1_OFF, smem, bid);
  else k1_wt(wr, wi, ws + WT_OFF, bid - 256);
}

constexpr int K2_W1_PITCH = 288, K2_W1_MAT = 80 * K2_W1_PITCH + 128;
constexpr int K2_LDS = 2 * K2_W1_MAT;
constexpr int K2_GRID = 81 + 128;

DI void k2_col(const char* __restrict__ X1, const char* __restrict__ WT, char* __restrict__ X2, int* __restrict__ ecol,
               char* smem, int wg) {
  const int tid = threadIdx.x, lane = tid & 63, w = wave_id(), r = lane & 31, h = lane >> 5;
  int a1, b1, am, bm;
  bool valid = true, direct = false, self = false;
  if (wg < 75) { a1 = wg / 5 + 1; b1 = 4 * (wg % 5) + w; am = 32 - a1; bm = 19 - b1; }
  else if (wg < 78) { a1 = 16; b1 = 4 * (wg - 75) + w; am = 16; bm = 19 - b1; valid = b1 < 10; }
  else { a1 = 0; b1 = 4 * (wg - 78) + w; am = 0; bm = b1 ? 20 - b1 : 0; valid = b1 <= 10; direct = true; self = (b1 == 0 || b1 == 10); }
  const int q1 = a1 + 32 * b1, qm = am + 32 * bm;
  const bool swp = q1 > 320;
  const int qA = swp ? qm : q1;
  if (valid) {
    const int slotU = 8 * wg + (swp ? 4 : 0) + w, slotV = 8 * wg + (swp ? 0 : 4) + w;
    const u32x4* xb = (const u32x4*)X1 + (size_t)(r * 8 + h) * 648;
    u32x4 au[4], av[4];
#pragma unroll
    for (int ks = 0; ks < 4; ++ks) { au[ks] = xb[2 * 648 * ks + slotU]; av[ks] = xb[2 * 648 * ks + slotV]; }
    u32x4 wv[8];
    const u32x4* wp = (const u32x4*)(WT + (size_t)qA * 8192) + lane;
#pragma unroll
    for (int i = 0; i < 8; ++i) wv[i] = wp[64 * i];

    f16x8 BR[4], BI[4];
#pragma unroll
    for (int ks = 0; ks < 4; ++ks)
#pragma unroll
      for (int i = 0; i < 4; ++i) {
        float rev = (float)(((8 * ks + 4 * h + i) * r) & 31) * (1.f / 32.f);
        float c = cosr(rev), sn = sinr(rev);
        BR[ks][2 * i] = (f16)c; BR[ks][2 * i + 1] = (f16)sn;
        BI[ks][2 * i] = (f16)(-sn); BI[ks][2 * i + 1] = (f16)c;
      }
    f16x8 Cp[2], Sp[2], Np[2];
    float tc[16], ts[16], thc[16], ths[16];
#pragma unroll
    for (int i = 0; i < 16; ++i) {
      const int k = crow(i, 0) + 4 * h;
      float rev = (float)((k * r) & 31) * (1.f / 32.f);
      float c = cosr(rev), sn = sinr(rev);
      Cp[i >> 3][i & 7] = (f16)c; Sp[i >> 3][i & 7] = (f16)sn; Np[i >> 3][i & 7] = (f16)(-sn);
      float rev2 = (float)(k * r) * (1.f / 1024.f);
      tc[i] = cosr(rev2) * (1.f / 32.f); ts[i] = sinr(rev2) * (1.f / 32.f);
      float rev3 = (float)(r + 32 * k) * (1.f / 2048.f);
      thc[i] = cosr(rev3); ths[i] = sinr(rev3);
    }
#pragma unroll
    for (int ks = 0; ks < 4; ++ks) av[ks] ^= 0x80000000u;

    f32x16 Ur = zero16(), Ui = zero16(), Vr = zero16(), Vi = zero16();
#pragma unroll
    for (int ks = 0; ks < 4; ++ks) { Ur = mf16(asf16x8(au[ks]), BR[ks], Ur); Ui = mf16(asf16x8(au[ks]), BI[ks], Ui); }
#pragma unroll
    for (int ks = 0; ks < 4; ++ks) { Vr = mf16(asf16x8(av[ks]), BR[ks], Vr); Vi = mf16(asf16x8(av[ks]), BI[ks], Vi); }
    f16x8 pur[2], pui[2], pvr[2], pvi[2];
    {
      f32x16 tr_, ti_;
#pragma unroll
      for (int i = 0; i < 16; ++i) { tr_[i] = Ur[i] * tc[i] + Ui[i] * ts[i]; ti_[i] = Ui[i] * tc[i] - Ur[i] * ts[i]; }
      pur[0] = pack8(tr_, 0); pur[1] = pack8(tr_, 1); pui[0] = pack8(ti_, 0); pui[1] = pack8(ti_, 1);
    }
    Ur = zero16(); Ui = zero16();
#pragma unroll
    for (int s = 0; s < 2; ++s) {
      Ur = mf16(Cp[s], pur[s], Ur); Ui = mf16(Cp[s], pui[s], Ui);
      Ur = mf16(Sp[s], pui[s], Ur); Ui = mf16(Np[s], pur[s], Ui);
    }
    {
      f32x16 tr_, ti_;
#pragma unroll
      for (int i = 0; i < 16; ++i) { tr_[i] = Vr[i] * tc[i] + Vi[i] * ts[i]; ti_[i] = Vi[i] * tc[i] - Vr[i] * ts[i]; }
      pvr[0] = pack8(tr_, 0); pvr[1] = pack8(tr_, 1); pvi[0] = pack8(ti_, 0); pvi[1] = pack8(ti_, 1);
    }
    Vr = zero16(); Vi = zero16();
#pragma unroll
    for (int s = 0; s < 2; ++s) {
      Vr = mf16(Cp[s], pvr[s], Vr); Vi = mf16(Cp[s], pvi[s], Vi);
      Vr = mf16(Sp[s], pvi[s], Vr); Vi = mf16(Np[s], pvr[s], Vi);
    }
    float w0r[16], w0i[16], w1r[16], w1i[16];
#pragma unroll
    for (int i = 0; i < 8; ++i) {
      w0r[2 * i] = bf_lo(wv[i][0]); w0i[2 * i] = bf_hi(wv[i][0]); w1r[2 * i] = bf_lo(wv[i][1]); w1i[2 * i] = bf_hi(wv[i][1]);
      w0r[2 * i + 1] = bf_lo(wv[i][2]); w0i[2 * i + 1] = bf_hi(wv[i][2]); w1r[2 * i + 1] = bf_lo(wv[i][3]); w1i[2 * i + 1] = bf_hi(wv[i][3]);
    }
    if (self) {
      const u32x4* wq = (const u32x4*)(WT + (size_t)(b1 ? 322 : 321) * 8192) + lane;
#pragma unroll
      for (int i = 0; i < 8; ++i) {
        u32x4 t = wq[64 * i];
        w0r[2 * i] = 0.5f * (w0r[2 * i] + bf_lo(t[0])); w0i[2 * i] = 0.5f * (w0i[2 * i] - bf_hi(t[0]));
        w1r[2 * i] = 0.5f * (w1r[2 * i] + bf_lo(t[1])); w1i[2 * i] = 0.5f * (w1i[2 * i] - bf_hi(t[1]));
        w0r[2 * i + 1] = 0.5f * (w0r[2 * i + 1] + bf_lo(t[2])); w0i[2 * i + 1] = 0.5f * (w0i[2 * i + 1] - bf_hi(t[2]));
        w1r[2 * i + 1] = 0.5f * (w1r[2 * i + 1] + bf_lo(t[3])); w1i[2 * i + 1] = 0.5f * (w1i[2 * i + 1] - bf_hi(t[3]));
      }
    }
    float mx = 0.f;
#pragma unroll
    for (int i = 0; i < 16; ++i) {
      const float sr = w0r[i] + w1r[i], si = w0i[i] + w1i[i], dr = w0r[i] - w1r[i], di = w0i[i] - w1i[i];
      const float dsr = dr * ths[i], dsi = di * ths[i], dcr = dr * thc[i], dci = di * thc[i];
      const float ar = sr - dsr, ai = si - dsi, br = sr + dsr, bi = si + dsi;
      const float ur = Ur[i], ui = Ui[i], vr = Vr[i], vi = Vi[i];
      const float nur = ar * ur - ai * ui - dcr * vi - dci * vr;
      const float nui = ar * ui + ai * ur + dcr * vr - dci * vi;
      const float nvr = br * vr - bi * vi + dcr * ui + dci * ur;
      const float nvi = br * vi + bi * vr - dcr * ur + dci * ui;
      Ur[i] = nur; Ui[i] = nui; Vr[i] = nvr; Vi[i] = nvi;
      mx = fmaxf(mx, fmaxf(fmaxf(fabsf(nur), fabsf(nui)), fmaxf(fabsf(nvr), fabsf(nvi))));
    }
    mx = max64(mx);
    int eb = (int)(asu(mx) >> 23);
    eb = eb < 8 ? 8 : eb;
    const float sc = asf((u32)(262 - eb) << 23);
    Ur *= sc; Ui *= sc; Vr *= sc; Vi *= sc;
    const int aU = swp ? am : a1, bU = swp ? bm : b1, aV = swp ? a1 : am, bV = swp ? b1 : bm;
    if (lane == 0) { ecol[aU * 20 + bU] = eb; ecol[aV * 20 + bV] = eb; }
    pur[0] = pack8(Ur, 0); pur[1] = pack8(Ur, 1); pui[0] = pack8(Ui, 0); pui[1] = pack8(Ui, 1);
    pvr[0] = pack8(Vr, 0); pvr[1] = pack8(Vr, 1); pvi[0] = pack8(Vi, 0); pvi[1] = pack8(Vi, 1);
    Ur = zero16(); Ui = zero16(); Vr = zero16(); Vi = zero16();
#pragma unroll
    for (int s = 0; s < 2; ++s) {
      Ur = mf16(pur[s], Cp[s], Ur); Ui = mf16(pur[s], Sp[s], Ui);
      Ur = mf16(pui[s], Np[s], Ur); Ui = mf16(pui[s], Cp[s], Ui);
    }
#pragma unroll
    for (int s = 0; s < 2; ++s) {
      Vr = mf16(pvr[s], Cp[s], Vr); Vi = mf16(pvr[s], Sp[s], Vi);
      Vr = mf16(pvi[s], Np[s], Vr); Vi = mf16(pvi[s], Cp[s], Vi);
    }
    {
      f32x16 tr_, ti_;
#pragma unroll
      for (int i = 0; i < 16; ++i) { tr_[i] = Ur[i] * tc[i] - Ui[i] * ts[i]; ti_[i] = Ui[i] * tc[i] + Ur[i] * ts[i]; }
      pur[0] = pack8(tr_, 0); pur[1] = pack8(tr_, 1); pui[0] = pack8(ti_, 0); pui[1] = pack8(ti_, 1);
    }
    Ur = zero16(); Ui = zero16();
#pragma unroll
    for (int s = 0; s < 2; ++s) {
      Ur = mf16(Cp[s], pur[s], Ur); Ui = mf16(Sp[s], pur[s], Ui);
      Ur = mf16(Np[s], pui[s], Ur); Ui = mf16(Cp[s], pui[s], Ui);
    }
    {
      f32x16 tr_, ti_;
#pragma unroll
      for (int i = 0; i < 16; ++i) { tr_[i] = Vr[i] * tc[i] - Vi[i] * ts[i]; ti_[i] = Vi[i] * tc[i] + Vr[i] * ts[i]; }
      pvr[0] = pack8(tr_, 0); pvr[1] = pack8(tr_, 1); pvi[0] = pack8(ti_, 0); pvi[1] = pack8(ti_, 1);
    }
    Vr = zero16(); Vi = zero16();
#pragma unroll
    for (int s = 0; s < 2; ++s) {
      Vr = mf16(Cp[s], pvr[s], Vr); Vi = mf16(Sp[s], pvr[s], Vi);
      Vr = mf16(Np[s], pvi[s], Vr); Vi = mf16(Cp[s], pvi[s], Vi);
    }
    if (!direct) {
      const int setU = (wg == 77) ? 0 : (swp ? 1 : 0), setV = (wg == 77) ? 0 : (swp ? 0 : 1);
      u32x4* su = (u32x4*)smem + ((setU * 4 + (bU & 3)) * 4) * 64 + lane;
      u32x4* sv = (u32x4*)smem + ((setV * 4 + (bV & 3)) * 4) * 64 + lane;
#pragma unroll
      for (int g = 0; g < 4; ++g) {
        u32x4 o = {pk_f16(Ur[4 * g], Ui[4 * g]), pk_f16(Ur[4 * g + 1], Ui[4 * g + 1]), pk_f16(Ur[4 * g + 2], Ui[4 * g + 2]),
                   pk_f16(Ur[4 * g + 3], Ui[4 * g + 3])};
        su[64 * g] = o;
      }
#pragma unroll
      for (int g = 0; g < 4; ++g) {
        u32x4 o = {pk_f16(Vr[4 * g], -Vi[4 * g]), pk_f16(Vr[4 * g + 1], -Vi[4 * g + 1]), pk_f16(Vr[4 * g + 2], -Vi[4 * g + 2]),
                   pk_f16(Vr[4 * g + 3], -Vi[4 * g + 3])};
        sv[64 * g] = o;
      }
    } else {
      u32* du = (u32*)(X2 + (size_t)(bU >> 2) * 16384) + (bU & 3) + 4 * (128 * h + r);
      u32* dv = (u32*)(X2 + (size_t)(bV >> 2) * 16384) + (bV & 3) + 4 * (128 * h + r);
#pragma unroll
      for (int i = 0; i < 16; ++i) du[128 * crow(i, 0)] = pk_f16(Ur[i], Ui[i]);
      if (!self) {
#pragma unroll
        for (int i = 0; i < 16; ++i) dv[128 * crow(i, 0)] = pk_f16(Vr[i], -Vi[i]);
      }
    }
  }
  if (!direct) {
    __syncthreads();
    const int c1 = (wg < 75) ? (wg % 5) : (wg - 75);
    const int nset = (wg == 77) ? 1 : 2;
    for (int set = 0; set < nset; ++set) {
      const int quad = set ? (am * 5 + 4 - c1) : (a1 * 5 + c1);
      const u32x4* sp = (const u32x4*)smem + (set * 16 + w) * 64 + lane;
      u32x4 p0 = sp[0], p1 = sp[256], p2 = sp[512], p3 = sp[768];
      u32x4* dp = (u32x4*)(X2 + (size_t)quad * 16384) + 32 * (8 * w + 4 * h) + r;
#pragma unroll
      for (int i = 0; i < 4; ++i) { u32x4 o = {p0[i], p1[i], p2[i], p3[i]}; dp[32 * i] = o; }
    }
  }
}

DI void k2_w1(const u16* __restrict__ wg_, const u16* __restrict__ wu_, char* __restrict__ W1, char* smem, int idx) {
  const int tid = threadIdx.x, lane = tid & 63, w = wave_id();
  const int t = idx >> 3, u = idx & 7;
  u32x2 v[17];
#pragma unroll
  for (int it = 0; it < 17; ++it) {
    const int e = tid + 256 * it;
    if (e < 4320) {
      const int row2 = e / 27, c8 = e - 27 * row2, mat = row2 >= 80, row = row2 - 80 * mat;
      const u16* base = mat ? wu_ : wg_;
      v[it] = *(const u32x2*)(base + (size_t)(80 * u + row) * 1728 + 108 * t + 4 * c8);
    }
  }
#pragma unroll
  for (int it = 0; it < 17; ++it) {
    const int e = tid + 256 * it;
    if (e < 4320) {
      const int row2 = e / 27, c8 = e - 27 * row2, mat = row2 >= 80, row = row2 - 80 * mat;
      *(u32x2*)(smem + mat * K2_W1_MAT + row * K2_W1_PITCH + 8 * c8) = v[it];
    }
  }
  if (tid < 160) { u32x2 z = {0u, 0u}; *(u32x2*)(smem + (tid >= 80) * K2_W1_MAT + (tid % 80) * K2_W1_PITCH + 216) = z; }
  __syncthreads();
  const int h2 = lane >> 5, blk = (lane >> 4) & 1, q4 = (lane & 15) >> 2, p4 = lane & 3;
  const char* lb = smem + blk * K2_W1_MAT + 8 * p4;
  for (int fr = w; fr < 35; fr += 4) {
    const int kk = fr / 7, ft = fr - 7 * kk;
    const int k16 = kk < 4 ? 4 * u + kk : 32 + u;
    const int r0 = kk < 4 ? 20 * kk + 4 * h2 + q4 : 40 * h2 + 16 + q4;
    const int r1 = kk < 4 ? r0 + 8 : r0 + 20;
    u32x2 lo = tr_read(lb + r0 * K2_W1_PITCH + 32 * ft), hi = tr_read(lb + r1 * K2_W1_PITCH + 32 * ft);
    const int pos = ft < JT ? k16 * JT + ft : 40 * JT + (ft - JT) * 40 + k16;
    u32x4 o = {lo[0], lo[1], hi[0], hi[1]};
    *((u32x4*)(W1 + ((size_t)t * 280 + pos) * 1024) + lane) = o;
  }
}

__global__ __launch_bounds__(256) void k2_kernel(const u16* __restrict__ wg_, const u16* __restrict__ wu_, char* __restrict__ ws) {
  __shared__ __attribute__((aligned(16))) char smem[K2_LDS];
  const int bid = blockIdx.x;
  if (bid < 81) k2_col(ws + X1_OFF, ws + WT_OFF, ws + X2_OFF, (int*)(ws + EC_OFF), smem, bid);
  else k2_w1(wg_, wu_, ws + W1_OFF, smem, bid - 81);
}

constexpr int K3_W2_PITCH = 672;
constexpr int K3_LDS = 32 * K3_W2_PITCH;
constexpr int K3_GRID = 256 + 112;

DI void k3_row(const char* __restrict__ X2, const int* __restrict__ ecol, const u16* __restrict__ x, u16* __restrict__ H,
               char* __restrict__ ACT, char* smem, int wg) {
  const int tid = threadIdx.x, lane = tid & 63, w = wave_id(), r = lane & 31, h = lane >> 5;
  const int m = 4 * wg + w;
  u32x4 a[3];
  i32x4 ec[3];
#pragma unroll
  for (int ks = 0; ks < 3; ++ks) {
    const int c = (ks == 2) ? 4 : 2 * ks + h;
    a[ks] = *((const u32x4*)X2 + (size_t)(r * 5 + c) * 1024 + m);
    ec[ks] = *((const i32x4*)ecol + r * 5 + c);
  }
  u32x2 xr[2][3];
#pragma unroll
  for (int rr = 0; rr < 2; ++rr)
#pragma unroll
    for (int g = 0; g < 3; ++g) {
      const int b0 = (g == 2) ? 16 : 8 * g + 4 * h;
      xr[rr][g] = *(const u32x2*)(x + (size_t)(2 * m + rr) * 640 + 20 * r + b0);
    }
  f16x8 BR[3], BI[3];
#pragma unroll
  for (int ks = 0; ks < 3; ++ks)
#pragma unroll
    for (int i = 0; i < 4; ++i) {
      const int bp = 8 * ks + 4 * h + i;
      float rev = (float)(bp * r) * 0.05f;
      const bool ok = (ks < 2) || (h == 0);
      float c = ok ? cosr(rev) : 0.f, sn = ok ? sinr(rev) : 0.f;
      BR[ks][2 * i] = (f16)c; BR[ks][2 * i + 1] = (f16)(-sn);
      BI[ks][2 * i] = (f16)sn; BI[ks][2 * i + 1] = (f16)c;
    }
  f16x8 Cp[2], Sp[2], Np[2];
  float tc[16], ts[16];
#pragma unroll
  for (int i = 0; i < 16; ++i) {
    const int k = crow(i, 0) + 4 * h;
    float rev = (float)((k * r) & 31) * (1.f / 32.f);
    float c = cosr(rev), sn = sinr(rev);
    Cp[i >> 3][i & 7] = (f16)c; Sp[i >> 3][i & 7] = (f16)sn; Np[i >> 3][i & 7] = (f16)(-sn);
    float rev2 = (float)(k * r) * (1.f / 640.f);
    tc[i] = cosr(rev2) * (1.f / 32.f); ts[i] = sinr(rev2) * (1.f / 32.f);
  }
  int em = 0;
#pragma unroll
  for (int ks = 0; ks < 3; ++ks)
#pragma unroll
    for (int i = 0; i < 4; ++i) em = max(em, ec[ks][i]);
  const int E = (int)max64((float)em);
#pragma unroll
  for (int ks = 0; ks < 3; ++ks)
#pragma unroll
    for (int i = 0; i < 4; ++i) {
      const f16 s = (f16)__builtin_amdgcn_ldexpf(1.f, ec[ks][i] - E);
      f16x2 s2 = {s, s};
      const u32 tmp = a[ks][i];
      const f16x2 pr2 = __builtin_bit_cast(f16x2, tmp) * s2;
      a[ks][i] = __builtin_bit_cast(u32, pr2);
    }
  const float fs = __builtin_amdgcn_ldexpf(1.f / 1280.f, E - 127);
  f32x16 Tr = zero16(), Ti = zero16();
#pragma unroll
  for (int ks = 0; ks < 3; ++ks) { Tr = mf16(asf16x8(a[ks]), BR[ks], Tr); Ti = mf16(asf16x8(a[ks]), BI[ks], Ti); }
  f16x8 pr[2], pi[2];
  {
    f32x16 tr_, ti_;
#pragma unroll
    for (int i = 0; i < 16; ++i) { tr_[i] = Tr[i] * tc[i] - Ti[i] * ts[i]; ti_[i] = Ti[i] * tc[i] + Tr[i] * ts[i]; }
    pr[0] = pack8(tr_, 0); pr[1] = pack8(tr_, 1); pi[0] = pack8(ti_, 0); pi[1] = pack8(ti_, 1);
  }
  f32x16 Vr = zero16(), Vi = zero16();
#pragma unroll
  for (int s = 0; s < 2; ++s) {
    Vr = mf16(pr[s], Cp[s], Vr); Vi = mf16(pr[s], Sp[s], Vi);
    Vr = mf16(pi[s], Np[s], Vr); Vi = mf16(pi[s], Cp[s], Vi);
  }
  float hv[2][12];
  u32 hp[2][6];
  float sm[2] = {0.f, 0.f};
#pragma unroll
  for (int rr = 0; rr < 2; ++rr)
#pragma unroll
    for (int k = 0; k < 6; ++k) {
      const float y0 = (rr ? Vi[2 * k] : Vr[2 * k]) * fs, y1 = (rr ? Vi[2 * k + 1] : Vr[2 * k + 1]) * fs;
      const u32 yb = pk_bf16(y0, y1);
      const u32 xx = xr[rr][k >> 1][k & 1];
      u32 hh = pk_bf16(bf_lo(xx) + bf_lo(yb), bf_hi(xx) + bf_hi(yb));
      if (k >= 4) hh = h ? 0u : hh;
      hp[rr][k] = hh;
      hv[rr][2 * k] = bf_lo(hh); hv[rr][2 * k + 1] = bf_hi(hh);
      sm[rr] += hv[rr][2 * k] + hv[rr][2 * k + 1];
    }
  float mean[2], q2[2] = {0.f, 0.f};
#pragma unroll
  for (int rr = 0; rr < 2; ++rr) mean[rr] = sum64(sm[rr]) * (1.f / 640.f);
#pragma unroll
  for (int rr = 0; rr < 2; ++rr) {
#pragma unroll
    for (int i = 0; i < 8; ++i) { float d = hv[rr][i] - mean[rr]; q2[rr] += d * d; }
    float q3 = 0.f;
#pragma unroll
    for (int i = 8; i < 12; ++i) { float d = hv[rr][i] - mean[rr]; q3 += d * d; }
    q2[rr] += h ? 0.f : q3;
  }
#pragma unroll
  for (int rr = 0; rr < 2; ++rr) {
    u16* hrow = H + (size_t)(2 * m + rr) * 640 + 20 * r;
    u32x2 o0 = {hp[rr][0], hp[rr][1]}, o1 = {hp[rr][2], hp[rr][3]}, o2 = {hp[rr][4], hp[rr][5]};
    *(u32x2*)(hrow + 4 * h) = o0;
    *(u32x2*)(hrow + 8 + 4 * h) = o1;
    if (h == 0) *(u32x2*)(hrow + 16) = o2;
  }
#pragma unroll
  for (int rr = 0; rr < 2; ++rr) {
    const float var = sum64(q2[rr]) * (1.f / 640.f);
    float mb, inv;
    ln_scalars(mean[rr], var, mb, inv);
    u32 o[6];
#pragma unroll
    for (int k = 0; k < 6; ++k) {
      const u32 d = pk_bf16(hv[rr][2 * k] - mb, hv[rr][2 * k + 1] - mb);
      o[k] = pk_bf16(bf_lo(d) * inv, bf_hi(d) * inv);
    }
    const int rowl = 2 * w + rr;
    u32x4 o4 = {o[0], o[1], o[2], o[3]};
    *(u32x4*)(smem + (h * 32 + r) * 144 + rowl * 16) = o4;
    if (h == 0) { u32x2 o2 = {o[4], o[5]}; *(u32x2*)(smem + (64 + (r >> 1)) * 144 + rowl * 16 + (r & 1) * 8) = o2; }
  }
  __syncthreads();
  const int row0 = 8 * wg;
  char* ab = ACT + (size_t)(row0 >> 5) * (40 * 1024) + ((row0 & 31) + (tid & 7)) * 16;
#pragma unroll
  for (int it = 0; it < 3; ++it) {
    const int gi = (tid >> 3) + 32 * it;
    if (gi < 80) {
      const int L = gi < 64 ? (gi & 1) * 32 + (gi >> 1) : gi;
      u32x4 o = *(const u32x4*)(smem + L * 144 + (tid & 7) * 16);
      *(u32x4*)(ab + gi * 512) = o;
    }
  }
}

DI void k3_w2(const u16* __restrict__ wd, char* __restrict__ W2, char* smem, int idx) {
  const int tid = threadIdx.x, lane = tid & 63, w = wave_id();
  const int k32 = idx >> 1, nh = idx & 1;
  u32x4 v[5];
#pragma unroll
  for (int it = 0; it < 5; ++it) {
    const int e = tid + 256 * it;
    const int row = e / 40, c = e - 40 * row;
    const int u2 = 2 * k32 + (row >> 4), t = u2 / 7, ft = u2 - 7 * t, fl = 16 * ft + (row & 15);
    u32x4 z = {0u, 0u, 0u, 0u};
    v[it] = fl < 108 ? *(const u32x4*)(wd + (size_t)(108 * t + fl) * 640 + 320 * nh + 8 * c) : z;
  }
#pragma unroll
  for (int it = 0; it < 5; ++it) {
    const int e = tid + 256 * it;
    const int row = e / 40, c = e - 40 * row;
    *(u32x4*)(smem + row * K3_W2_PITCH + 16 * c) = v[it];
  }
  __syncthreads();
  const int g = lane >> 4, q4 = (lane & 15) >> 2, p4 = lane & 3;
  const char* lb = smem + (16 * (g >> 1) + 4 * (g & 1) + q4) * K3_W2_PITCH + 8 * p4;
#pragma unroll
  for (int i = 0; i < 5; ++i) {
    const int fr = w * 5 + i;
    u32x2 lo = tr_read(lb + 32 * fr), hi = tr_read(lb + 8 * K3_W2_PITCH + 32 * fr);
    u32x4 o = {lo[0], lo[1], hi[0], hi[1]};
    *((u32x4*)(W2 + ((size_t)((4 * nh + w) * 56 + k32) * 5 + i) * 1024) + lane) = o;
  }
}

__global__ __launch_bounds__(256) void k3_kernel(const u16* __restrict__ x, const u16* __restrict__ wd, char* __restrict__ ws) {
  __shared__ __attribute__((aligned(16))) char smem[K3_LDS];
  const int bid = blockIdx.x;
  if (bid < 256) k3_row(ws + X2_OFF, (const int*)(ws + EC_OFF), x, (u16*)(ws + H_OFF), ws + ACT_OFF, smem, bid);
  else k3_w2(wd, ws + W2_OFF, smem, bid - 256);
}

template <int N> struct IC { static constexpr int v = N; };
template <int I, int N, class F> DI void static_for(F&& f) {
  if constexpr (I < N) { f(IC<I>{}); static_for<I + 1, N>(f); }
}
template <int N> DI void wait_vm() { asm volatile("s_waitcnt vmcnt(%0)" ::"n"(N) : "memory"); }
DI void raw_bar() { asm volatile("s_barrier" ::: "memory"); }

template <int N> DI void dma(u32 voff, const char* g, u32 lds) {
  u32 keep;
  if constexpr (N == 4)
    asm volatile("s_mov_b32 %0, m0\n\ts_mov_b32 m0, %3\n\ts_nop 0\n\tglobal_load_lds_dwordx4 %1, %2\n\t"
                 "global_load_lds_dwordx4 %1, %2 offset:1024\n\tglobal_load_lds_dwordx4 %1, %2 offset:2048\n\t"
                 "global_load_lds_dwordx4 %1, %2 offset:3072\n\ts_mov_b32 m0, %0"
                 : "=&s"(keep) : "v"(voff), "s"(g), "s"(lds) : "memory");
  else if constexpr (N == 2)
    asm volatile("s_mov_b32 %0, m0\n\ts_mov_b32 m0, %3\n\ts_nop 0\n\tglobal_load_lds_dwordx4 %1, %2\n\t"
                 "global_load_lds_dwordx4 %1, %2 offset:1024\n\ts_mov_b32 m0, %0"
                 : "=&s"(keep) : "v"(voff), "s"(g), "s"(lds) : "memory");
  else
    asm volatile("s_mov_b32 %0, m0\n\ts_mov_b32 m0, %3\n\ts_nop 0\n\tglobal_load_lds_dwordx4 %1, %2\n\ts_mov_b32 m0, %0"
                 : "=&s"(keep) : "v"(voff), "s"(g), "s"(lds) : "memory");
}

DI float geglu1(float g, float u) {
  const float k1 = -2.3022081983f, k2 = -0.1029432397f;
  const float z = g * (k1 + k2 * g * g);
  return g * __builtin_amdgcn_rcpf(1.f + __builtin_amdgcn_exp2f(z));
}

constexpr int K4_SLOT = 40 * 1024;
constexpr int K4_P1 = 40;
constexpr int K4_KS = 40 / (4 + JT);
constexpr int K4_NS1 = 40 / K4_KS;
constexpr int K4_NSTAGE = K4_NS1 + (7 - JT);
constexpr int K4_NSTEP = 40 + 10 * (7 - JT);
static_assert(JT == 4, "step structure assumes 4 MFMAs per step");

__global__ __launch_bounds__(256, 1) void k4_kernel(char* __restrict__ ws) {
  __shared__ __attribute__((aligned(16))) char smem[4 * K4_SLOT];
  const int tid = threadIdx.x, lane = tid & 63, w = wave_id(), r = lane & 31, h = lane >> 5;
  const int bid = blockIdx.x, xcd = bid & 7, idx = bid >> 3;
  const int rb = 8 * (xcd & 1) + (idx & 7), t = 4 * (xcd >> 1) + (idx >> 3);
  const char* actg = ws + ACT_OFF + (size_t)(4 * rb + w) * (40 * 1024);
  const char* w1g = ws + W1_OFF + (size_t)t * (280 * 1024);
  const u32 voff = lane * 16;
  const u32 lds0 = (u32)(size_t)(LAS char*)smem;

  auto issue = [&](auto S) {
    constexpr int s = decltype(S)::v;
    const u32 slot = lds0 + (s & 3) * K4_SLOT;
    if constexpr (s < K4_NS1) {
      const char* ga = actg + s * (K4_KS * 1024);
      dma<4>(voff, ga, slot + w * 5120); dma<1>(voff, ga + 4096, slot + w * 5120 + 4096);
      const char* gw = w1g + s * 20480 + w * 5120;
      dma<4>(voff, gw, slot + 20480 + w * 5120); dma<1>(voff, gw + 4096, slot + 20480 + w * 5120 + 4096);
    } else {
      const char* gw = w1g + 160 * 1024 + (s - K4_NS1) * 40960 + w * 10240;
      dma<4>(voff, gw, slot + w * 10240); dma<4>(voff, gw + 4096, slot + w * 10240 + 4096);
      dma<2>(voff, gw + 8192, slot + w * 10240 + 8192);
    }
  };
  static_for<0, 4>(issue);

  const char* lw = smem + lane * 16;
  const char* la = smem + lane * 16 + w * 5120;
  u32x4 act[40];
  u32x4 wf[2][4];
  f32x16 acc[7];
#pragma unroll
  for (int i = 0; i < 7; ++i) acc[i] = zero16();
  u32 gout[7][4];

  auto rd = [&](auto I) {
    constexpr int i = decltype(I)::v;
    if constexpr (i < K4_P1) {
      constexpr int s = i / K4_KS, kl = i % K4_KS, so = (s & 3) * K4_SLOT;
      act[i] = *(const u32x4*)(la + so + kl * 1024);
#pragma unroll
      for (int c = 0; c < 4; ++c) wf[i & 1][c] = *(const u32x4*)(lw + so + 20480 + (kl * 4 + c) * 1024);
    } else {
      constexpr int s = K4_NS1 + (i - K4_P1) / 10, u = (i - K4_P1) % 10, so = (s & 3) * K4_SLOT;
#pragma unroll
      for (int c = 0; c < 4; ++c) wf[i & 1][c] = *(const u32x4*)(lw + so + (u * 4 + c) * 1024);
    }
  };
  const int m = 128 * rb + 32 * w + r;
  char* gdst = ws + G_OFF + ((size_t)((m >> 6) * 56) * 4 + ((m >> 4) & 3)) * 1024 + (m & 15) * 16;
  auto epi_pair = [&](auto FT, auto P) {
    constexpr int ft = decltype(FT)::v, p = decltype(P)::v;
    const u32 gb = pk_bf16(acc[ft][2 * p], acc[ft][2 * p + 1]);
    const u32 ub = pk_bf16(acc[ft][8 + 2 * p], acc[ft][8 + 2 * p + 1]);
    const u32 ge = pk_bf16(geglu1(bf_lo(gb), 0.f), geglu1(bf_hi(gb), 0.f));
    gout[ft][p] = pk_bf16(bf_lo(ge) * bf_lo(ub), bf_hi(ge) * bf_hi(ub));
  };
  auto epi_store = [&](auto FT) {
    constexpr int ft = decltype(FT)::v;
    const int gam = 14 * t + 2 * ft + h;
    u32x4 o = {gout[ft][0], gout[ft][1], gout[ft][2], gout[ft][3]};
    *(u32x4*)(gdst + (gam >> 2) * 4096 + (gam & 3) * 256) = o;
  };

  wait_vm<30>();
  raw_bar();
  rd(IC<0>{});
  static_for<0, K4_NSTEP>([&](auto I) {
    constexpr int i = decltype(I)::v;
    constexpr int nx = i + 1;
    if constexpr (nx < K4_NSTEP) {
      constexpr bool bound = (nx < K4_P1) ? (nx % K4_KS == 0) : ((nx - K4_P1) % 10 == 0);
      if constexpr (bound) {
        constexpr int sn = (nx < K4_P1) ? nx / K4_KS : K4_NS1 + (nx - K4_P1) / 10;
        constexpr int younger = (K4_NSTAGE - 1 - sn) < 2 ? (K4_NSTAGE - 1 - sn) : 2;
        asm volatile("s_waitcnt lgkmcnt(0)" ::: "memory");
        wait_vm<10 * younger>();
        raw_bar();
        if constexpr (sn + 3 < K4_NSTAGE) issue(IC<sn + 3>{});
      }
      rd(IC<nx>{});
    }
    if constexpr (i < K4_P1) {
#pragma unroll
      for (int c = 0; c < 4; ++c) acc[c] = mb32(wf[i & 1][c], act[i], acc[c]);
    } else {
      constexpr int ft = JT + (i - K4_P1) / 10, u = (i - K4_P1) % 10;
#pragma unroll
      for (int c = 0; c < 4; ++c) acc[ft] = mb32(wf[i & 1][c], act[4 * u + c], acc[ft]);
      constexpr int e = (i - K4_P1) / 5, ph = (i - K4_P1) % 5;
      if constexpr (ph < 4) epi_pair(IC<e>{}, IC<ph>{});
      else epi_store(IC<e>{});
    }
    __builtin_amdgcn_sched_barrier(0);
  });
  static_for<0, 4>([&](auto P) { epi_pair(IC<6>{}, P); });
  epi_store(IC<6>{});
}

constexpr int K5_DEPTH = 7;
__global__ __launch_bounds__(256, 1) void k5_kernel(const char* __restrict__ ws, u16* __restrict__ out) {
  __shared__ __attribute__((aligned(16))) char smem[4 * 20 * 1024];
  const int tid = threadIdx.x, lane = tid & 63, w = wave_id();
  const int bid = blockIdx.x, xcd = bid & 7, idx = bid >> 3;
  const int mb = 8 * (xcd >> 1) + (idx & 7), nb = 4 * (xcd & 1) + (idx >> 3);
  const u32x4* gp = (const u32x4*)(ws + G_OFF) + ((size_t)(mb * 56 + 14 * w) * 4) * 64 + lane;
  const u32x4* wp = (const u32x4*)(ws + W2_OFF) + ((size_t)(nb * 56 + 14 * w) * 5) * 64 + lane;
  u32x4 gb[K5_DEPTH][4], wb[K5_DEPTH][5];
#pragma unroll
  for (int s = 0; s < K5_DEPTH; ++s) {
#pragma unroll
    for (int i = 0; i < 4; ++i) gb[s][i] = gp[(s * 4 + i) * 64];
#pragma unroll
    for (int i = 0; i < 5; ++i) wb[s][i] = wp[(s * 5 + i) * 64];
  }
  __builtin_amdgcn_sched_barrier(0);
  const int mrow = 64 * mb + 16 * w + (lane & 15), ncol = 80 * nb + 4 * (lane >> 4);
  f32x4 acc[4][5];
#pragma unroll
  for (int i = 0; i < 4; ++i)
#pragma unroll
    for (int j = 0; j < 5; ++j) acc[i][j] = f32x4{0.f, 0.f, 0.f, 0.f};
#pragma unroll
  for (int s = 0; s < 14; ++s) {
    const int b = s % K5_DEPTH;
#pragma unroll
    for (int i = 0; i < 4; ++i)
#pragma unroll
      for (int j = 0; j < 5; ++j) acc[i][j] = mb16(wb[b][j], gb[b][i], acc[i][j]);
    if (s + K5_DEPTH < 14) {
#pragma unroll
      for (int i = 0; i < 4; ++i) gb[b][i] = gp[((s + K5_DEPTH) * 4 + i) * 64];
#pragma unroll
      for (int i = 0; i < 5; ++i) wb[b][i] = wp[((s + K5_DEPTH) * 5 + i) * 64];
    }
    __builtin_amdgcn_sched_barrier(0);
  }
  const u16* hp = (const u16*)(ws + H_OFF) + (size_t)mrow * 640 + ncol;
  u32x2 hv[5];
#pragma unroll
  for (int j = 0; j < 5; ++j) hv[j] = *(const u32x2*)(hp + 16 * j);
  f32x4* red = (f32x4*)smem;
#pragma unroll
  for (int i = 0; i < 4; ++i)
    if (i != w) {
#pragma unroll
      for (int j = 0; j < 5; ++j) red[((w * 4 + i) * 5 + j) * 64 + lane] = acc[i][j];
    }
  __syncthreads();
  f32x4 own[5];
#pragma unroll
  for (int j = 0; j < 5; ++j) own[j] = w == 0 ? acc[0][j] : (w == 1 ? acc[1][j] : (w == 2 ? acc[2][j] : acc[3][j]));
#pragma unroll
  for (int sw = 0; sw < 4; ++sw)
    if (sw != w) {
#pragma unroll
      for (int j = 0; j < 5; ++j) own[j] += red[((sw * 4 + w) * 5 + j) * 64 + lane];
    }
  u16* op = out + (size_t)mrow * 640 + ncol;
#pragma unroll
  for (int j = 0; j < 5; ++j) {
    const u32 o0 = pk_bf16(own[j][0], own[j][1]), o1 = pk_bf16(own[j][2], own[j][3]);
    u32x2 o = {pk_bf16(bf_lo(hv[j][0]) + bf_lo(o0), bf_hi(hv[j][0]) + bf_hi(o0)),
               pk_bf16(bf_lo(hv[j][1]) + bf_lo(o1), bf_hi(hv[j][1]) + bf_hi(o1))};
    *(u32x2*)(op + 16 * j) = o;
  }
}

extern "C" void kernel_launch(void* const* d_in, const int* in_sizes, int n_in, void* d_out, int out_size, void* d_ws,
                              size_t ws_size, hipStream_t stream) {
  const u16* x = (const u16*)d_in[0];
  const u16* wr = (const u16*)d_in[1];
  const u16* wi = (const u16*)d_in[2];
  const u16* wg = (const u16*)d_in[4];
  const u16* wu = (const u16*)d_in[5];
  const u16* wd = (const u16*)d_in[6];
  char* ws = (char*)d_ws;
  k1_kernel<<<K1_GRID, 256, 0, stream>>>(x, wr, wi, ws);
  k2_kernel<<<K2_GRID, 256, 0, stream>>>(wg, wu, ws);
  k3_kernel<<<K3_GRID, 256, 0, stream>>>(x, wd, ws);
  k4_kernel<<<256, 256, 0, stream>>>(ws);
  k5_kernel<<<256, 256, 0, stream>>>(ws, (u16*)d_out);
}
```

```cpp
#include <hip/hip_runtime.h>
#include <hip/hip_bf16.h>
#include <stdint.h>

#define DI __device__ __forceinline__
typedef unsigned short u16;
typedef uint32_t u32;
typedef _Float16 f16;
typedef f16 f16x2 __attribute__((ext_vector_type(2)));
typedef f16 f16x4 __attribute__((ext_vector_type(4)));
typedef f16 f16x8 __attribute__((ext_vector_type(8)));
typedef __bf16 bf16x2 __attribute__((ext_vector_type(2)));
typedef __bf16 bf16x8 __attribute__((ext_vector_type(8)));
typedef short s16x4 __attribute__((ext_vector_type(4)));
typedef float f32x2 __attribute__((ext_vector_type(2)));
typedef float f32x4 __attribute__((ext_vector_type(4)));
typedef float f32x16 __attribute__((ext_vector_type(16)));
typedef u32 u32x2 __attribute__((ext_vector_type(2)));
typedef u32 u32x4 __attribute__((ext_vector_type(4)));
typedef int i32x4 __attribute__((ext_vector_type(4)));
#define LAS __attribute__((address_space(3)))

constexpr int NCOLWG = 162, NSLOT = 4 * NCOLWG;
constexpr size_t X1_OFF = 0;
constexpr size_t X1_SZ = 256ull * NSLOT * 16;
constexpr size_t WT_OFF = X1_OFF + X1_SZ;
constexpr size_t WT_SZ = 323ull * 8192;
constexpr size_t X2_OFF = WT_OFF + WT_SZ;
constexpr size_t X2_SZ = 160ull * 16384;
constexpr size_t EC_OFF = X2_OFF + X2_SZ;
constexpr size_t EC_SZ = 4096;
constexpr size_t H_OFF = EC_OFF + EC_SZ;
constexpr size_t H_SZ = 2048ull * 640 * 2;
constexpr size_t ACT_OFF = H_OFF + H_SZ;
constexpr size_t ACT_SZ = 64ull * 40 * 1024;
constexpr size_t W1_OFF = ACT_OFF + ACT_SZ;
constexpr size_t W1_SZ = 16ull * 280 * 1024;
constexpr size_t W2_OFF = W1_OFF + W1_SZ;
constexpr size_t W2_SZ = 8ull * 56 * 5 * 1024;
constexpr size_t G_OFF = W2_OFF + W2_SZ;
constexpr size_t G_SZ = 32ull * 56 * 4 * 1024;

#define STAMPN(ws_, k_, i_) do {} while (0)
#define STAMPF(ws_, k_, i_) do {} while (0)
#define STAMP(ws_, k_, i_) do {} while (0)
#define STAMPE(ws_, k_) do {} while (0)
DI float asf(u32 u) { return __builtin_bit_cast(float, u); }
DI u32 asu(float f) { return __builtin_bit_cast(u32, f); }
DI u32 pk_bf16(float a, float b) { f32x2 v = {a, b}; return __builtin_bit_cast(u32, __builtin_convertvector(v, bf16x2)); }
DI u32 pk_f16(float a, float b) { f32x2 v = {a, b}; return __builtin_bit_cast(u32, __builtin_convertvector(v, f16x2)); }
DI u32 pk_f16v(f32x2 v) { return __builtin_bit_cast(u32, __builtin_convertvector(v, f16x2)); }
DI float bf_lo(u32 u) { return asf(u << 16); }
DI float bf_hi(u32 u) { return asf(u & 0xffff0000u); }
DI float bfr(float a) { return bf_lo(pk_bf16(a, a)); }
DI float cosr(float rev) { return __builtin_amdgcn_cosf(rev); }
DI float sinr(float rev) { return __builtin_amdgcn_sinf(rev); }
constexpr __host__ __device__ int crow(int reg, int h) { return (reg & 3) + 8 * (reg >> 2) + 4 * h; }
DI u32 umax(u32 a, u32 b) { return a > b ? a : b; }

template <int CTRL> DI u32 dppu(u32 v) { return (u32)__builtin_amdgcn_update_dpp(0, (int)v, CTRL, 0xf, 0xf, true); }
template <int CTRL> DI float dpp(float v) { return asf(dppu<CTRL>(asu(v))); }
DI float sum32(float v) {
  v += dpp<0xB1>(v); v += dpp<0x4E>(v); v += dpp<0x141>(v); v += dpp<0x140>(v);
  auto r = __builtin_amdgcn_permlane16_swap(asu(v), asu(v), false, false);
  return asf(r[0]) + asf(r[1]);
}
DI float sum64(float v) {
  v = sum32(v);
  auto r = __builtin_amdgcn_permlane32_swap(asu(v), asu(v), false, false);
  return asf(r[0]) + asf(r[1]);
}
DI u32 max64(u32 v) {
  v = umax(v, dppu<0xB1>(v)); v = umax(v, dppu<0x4E>(v)); v = umax(v, dppu<0x141>(v)); v = umax(v, dppu<0x140>(v));
  auto r = __builtin_amdgcn_permlane16_swap(v, v, false, false);
  v = umax(r[0], r[1]);
  auto s = __builtin_amdgcn_permlane32_swap(v, v, false, false);
  return umax(s[0], s[1]);
}
DI f16x8 asf16x8(u32x4 v) { return __builtin_bit_cast(f16x8, v); }
DI f32x16 mf16(u32x4 a, u32x4 b, f32x16 c) { return __builtin_amdgcn_mfma_f32_32x32x16_f16(asf16x8(a), asf16x8(b), c, 0, 0, 0); }
DI f32x16 mb32(u32x4 a, u32x4 b, f32x16 c) {
  return __builtin_amdgcn_mfma_f32_32x32x16_bf16(__builtin_bit_cast(bf16x8, a), __builtin_bit_cast(bf16x8, b), c, 0, 0, 0);
}
DI f32x4 mb16(u32x4 a, u32x4 b, f32x4 c) {
  return __builtin_amdgcn_mfma_f32_16x16x32_bf16(__builtin_bit_cast(bf16x8, a), __builtin_bit_cast(bf16x8, b), c, 0, 0, 0);
}
DI u32x2 tr_read(const char* p) {
  s16x4 r = __builtin_amdgcn_ds_read_tr16_b64_v4i16((LAS s16x4*)(p));
  return __builtin_bit_cast(u32x2, r);
}
DI f32x16 zero16() { f32x16 z; for (int i = 0; i < 16; ++i) z[i] = 0.f; return z; }
DI int wave_id() { return __builtin_amdgcn_readfirstlane(threadIdx.x >> 6); }
DI f32x2 lo2(const f32x16& v, int i) { return f32x2{v[i], v[i + 1]}; }
DI f32x2 pr2(const f32x4& v, int i) { return f32x2{v[i], v[i + 1]}; }
DI u32 pkmul_h(u32 a, u32 b) {
  const f16x2 r = __builtin_bit_cast(f16x2, a) * __builtin_bit_cast(f16x2, b);
  return __builtin_bit_cast(u32, r);
}
DI u32 pow2_h2(int d) {
  const float s = __builtin_amdgcn_ldexpf(1.f, d);
  return pk_f16(s, s);
}
template <class T> DI void pin(T& v) { asm volatile("" : "+v"(v)); }

DI void ln_scalars(float mean, float var, float& mb, float& inv) {
  mb = bfr(mean);
  float vb = bfr(var);
  float t = bfr(vb + 1e-5f);
  float den = bfr(__builtin_amdgcn_sqrtf(t));
  inv = __builtin_amdgcn_rcpf(den);
}

template <int N> struct IC { static constexpr int v = N; };
template <int I, int N, class F> DI void static_for(F&& f) {
  if constexpr (I < N) { f(IC<I>{}); static_for<I + 1, N>(f); }
}
template <int N> DI void wait_vm() { asm volatile("s_waitcnt vmcnt(%0)" ::"n"(N) : "memory"); }
DI void wait_lgkm0() { asm volatile("s_waitcnt lgkmcnt(0)" ::: "memory"); }
DI void raw_bar() { asm volatile("s_barrier" ::: "memory"); }
DI u32 lds_addr(const char* p) { return (u32)(size_t)(LAS const char*)p; }

template <int N> DI void dma(u32 voff, const void* g, u32 lds) {
  if constexpr (N == 4)
    asm volatile("s_mov_b32 m0, %2\n\ts_nop 0\n\tglobal_load_lds_dwordx4 %0, %1\n\t"
                 "global_load_lds_dwordx4 %0, %1 offset:1024\n\tglobal_load_lds_dwordx4 %0, %1 offset:2048\n\t"
                 "global_load_lds_dwordx4 %0, %1 offset:3072" ::"v"(voff), "s"(g), "s"(lds) : "memory");
  else if constexpr (N == 3)
    asm volatile("s_mov_b32 m0, %2\n\ts_nop 0\n\tglobal_load_lds_dwordx4 %0, %1\n\t"
                 "global_load_lds_dwordx4 %0, %1 offset:1024\n\tglobal_load_lds_dwordx4 %0, %1 offset:2048" ::"v"(voff),
                 "s"(g), "s"(lds) : "memory");
  else if constexpr (N == 2)
    asm volatile("s_mov_b32 m0, %2\n\ts_nop 0\n\tglobal_load_lds_dwordx4 %0, %1\n\t"
                 "global_load_lds_dwordx4 %0, %1 offset:1024" ::"v"(voff), "s"(g), "s"(lds) : "memory");
  else
    asm volatile("s_mov_b32 m0, %2\n\ts_nop 0\n\tglobal_load_lds_dwordx4 %0, %1" ::"v"(voff), "s"(g), "s"(lds) : "memory");
}
DI void touch35k(const char* g, int tid, u32 lds) {
  asm volatile("s_mov_b32 m0, %3\n\ts_nop 0\n\tglobal_load_lds_dword %0, %1\n\tglobal_load_lds_dword %2, %1" ::"v"((u32)tid * 128u),
               "s"(g), "v"((u32)(256 + min(tid, 23)) * 128u), "s"(lds) : "memory");
}

constexpr double PI_ = 3.14159265358979323846;
constexpr double ccos_(double x) {
  double t = 1, s = 1;
  for (int k = 1; k <= 16; ++k) { t *= -x * x / ((2 * k - 1) * (2 * k)); s += t; }
  return s;
}
constexpr double c2048(int k) { k &= 2047; k = k > 1024 ? 2048 - k : k; return ccos_(2 * PI_ * k / 2048); }
constexpr u32 fb(double x) { return __builtin_bit_cast(u32, (float)x); }

struct ColTask { int a1, b1, am, bm; bool valid; };
constexpr __host__ __device__ ColTask col_task(int g, int p) {
  if (g < 150) { const int a1 = g / 10 + 1, b1 = 2 * (g % 10) + p; return {a1, b1, 32 - a1, 19 - b1, true}; }
  if (g < 155) { const int b1 = 2 * (g - 150) + p; return {16, b1, 16, 19 - b1, true}; }
  if (g == 155) return {0, 0, 0, 0, p == 0};
  if (g == 156) return {0, 10, 0, 10, p == 0};
  const int b1 = 2 * (g - 157) + 1 + p;
  return {0, b1 < 10 ? b1 : 9, 0, b1 < 10 ? 20 - b1 : 11, b1 < 10};
}
struct SlotMap {
  u32 v[768];
  constexpr SlotMap() : v() {
    for (int slot = 0; slot < NSLOT; ++slot) {
      const int mir = (slot >> 1) & 1;
      const ColTask t = col_task(slot >> 2, slot & 1);
      const int q1 = t.a1 + 32 * t.b1, a = mir ? t.am : t.a1, b = mir ? t.bm : t.b1;
      const bool cj = mir ? !(q1 > 320) : (q1 > 320);
      v[slot] = (u32)(33 * b + a) | (cj ? 0x80000000u : 0u);
    }
  }
};
template <int SD> struct ThetaTab {
  u32 v[4 * 256];
  constexpr ThetaTab() : v() {
    for (int l = 0; l < 64; ++l)
      for (int n = 0; n < 8; ++n) {
        const int p = 16 * SD + (l & 15) + 32 * crow(4 * (n >> 1) + 2 * ((l >> 4) & 1) + (n & 1), l >> 5);
        v[((n >> 2) * 64 + l) * 4 + (n & 3)] = fb(c2048(p));
        v[((2 + (n >> 2)) * 64 + l) * 4 + (n & 3)] = fb(c2048(p + 1536));
      }
  }
};
struct Theta { ThetaTab<0> a; ThetaTab<1> b; };
static __device__ const __attribute__((aligned(128))) SlotMap g_smap{};
static __device__ const __attribute__((aligned(128))) Theta g_theta{};

DI void dft32_consts(float (&c)[16], float (&s)[16], int r, int h) {
  const float rf = (float)r * (1.f / 32.f), r4 = rf * (float)(4 * h);
#pragma unroll
  for (int i = 0; i < 16; ++i) { const float rev = rf * (float)crow(i, 0) + r4; c[i] = cosr(rev); s[i] = sinr(rev); }
}
DI void pack_csn(const float (&c)[16], const float (&s)[16], u32x4 (&Cp)[2], u32x4 (&Sp)[2], u32x4 (&Np)[2]) {
#pragma unroll
  for (int i = 0; i < 8; ++i) {
    Cp[i >> 2][i & 3] = pk_f16(c[2 * i], c[2 * i + 1]);
    Sp[i >> 2][i & 3] = pk_f16(s[2 * i], s[2 * i + 1]);
    Np[i >> 2][i & 3] = pk_f16(-s[2 * i], -s[2 * i + 1]);
  }
}

constexpr int K1_IMG = 1344;
constexpr int K1_PLANE = 33 * 20 * 4;
constexpr int K1_PL0 = 8 * K1_IMG;
constexpr int K1_LDS = 22528;
constexpr int K1_WT_WGS = 128;
constexpr int K1_GRID = 256 + K1_WT_WGS;
static_assert(K1_PL0 + 4 * K1_PLANE <= K1_LDS, "LDS");

DI void k1_row(const u16* __restrict__ x, char* __restrict__ X1, char* smem, int wg) {
  const int tid = threadIdx.x, lane = tid & 63, w = wave_id(), r = lane & 31, h = lane >> 5;
  const int b2 = wg >> 3, aq = wg & 7;
  const int m = 32 * (4 * aq + w) + b2;
  u32x2 v[5];
  u32 e[3];
  {
    const char* src = (const char*)x + (size_t)m * 2560;
    const u32 vo = h * 1280 + r * 8;
    asm volatile("global_load_dwordx2 %0, %5, %6\n\tglobal_load_dwordx2 %1, %5, %6 offset:256\n\t"
                 "global_load_dwordx2 %2, %5, %6 offset:512\n\tglobal_load_dwordx2 %3, %5, %6 offset:768\n\t"
                 "global_load_dwordx2 %4, %5, %6 offset:1024"
                 : "=&v"(v[0]), "=&v"(v[1]), "=&v"(v[2]), "=&v"(v[3]), "=&v"(v[4]) : "v"(vo), "s"(src) : "memory");
    asm volatile("global_load_dword %0, %3, %4\n\tglobal_load_dword %1, %3, %4 offset:1024\n\t"
                 "global_load_dword %2, %3, %4 offset:2048"
                 : "=&v"(e[0]), "=&v"(e[1]), "=&v"(e[2]) : "v"(tid * 4), "s"(g_smap.v) : "memory");
  }
  u32x4 C1[2], S1[2], N1[2], C2, S2, N2, MR, MI;
  f32x2 tc[6], ts[6];
  {
    float c[16], s[16];
    dft32_consts(c, s, r, h);
    pack_csn(c, s, C1, S1, N1);
    const float rf = (float)r * (1.f / 640.f), r4 = rf * (float)(4 * h);
#pragma unroll
    for (int i = 0; i < 12; ++i) {
      const float rev = rf * (float)crow(i, 0) + r4;
      tc[i >> 1][i & 1] = cosr(rev); ts[i >> 1][i & 1] = sinr(rev);
      const float rev2 = rev * 32.f;
      c[i] = cosr(rev2); s[i] = sinr(rev2);
    }
#pragma unroll
    for (int i = 0; i < 4; ++i) {
      C2[i] = pk_f16(c[2 * i], c[2 * i + 1]); S2[i] = pk_f16(s[2 * i], s[2 * i + 1]); N2[i] = pk_f16(-s[2 * i], -s[2 * i + 1]);
    }
#pragma unroll
    for (int i = 0; i < 2; ++i) {
      const u32 cc = h ? 0u : pk_f16(c[8 + 2 * i], c[9 + 2 * i]), ss = h ? 0u : pk_f16(s[8 + 2 * i], s[9 + 2 * i]);
      MR[i] = cc; MR[2 + i] = ss; MI[i] = ss ^ 0x80008000u; MI[2 + i] = cc;
    }
#pragma unroll
    for (int i = 0; i < 2; ++i) { pin(C1[i]); pin(S1[i]); pin(N1[i]); }
    pin(C2); pin(S2); pin(N2); pin(MR); pin(MI);
#pragma unroll
    for (int i = 0; i < 6; ++i) { pin(tc[i]); pin(ts[i]); }
  }
  STAMP(X1 - X1_OFF, 1, 5);
  asm volatile("s_waitcnt vmcnt(3)" : "+v"(v[0]), "+v"(v[1]), "+v"(v[2]), "+v"(v[3]), "+v"(v[4])::"memory");
  STAMP(X1 - X1_OFF, 1, 1);
  f32x2 f[10];
#pragma unroll
  for (int i = 0; i < 5; ++i) {
    f[2 * i] = f32x2{bf_lo(v[i][0]), bf_hi(v[i][0])};
    f[2 * i + 1] = f32x2{bf_lo(v[i][1]), bf_hi(v[i][1])};
  }
  f32x2 s2 = f[0];
#pragma unroll
  for (int i = 1; i < 10; ++i) s2 += f[i];
  const float mean = sum32(s2[0] + s2[1]) * (1.f / 640.f);
  f32x2 q2 = {0.f, 0.f};
#pragma unroll
  for (int i = 0; i < 10; ++i) { const f32x2 d = f[i] - mean; q2 += d * d; }
  const float var = sum32(q2[0] + q2[1]) * (1.f / 640.f);
  float mb, inv;
  ln_scalars(mean, var, mb, inv);
  char* img = smem + w * (2 * K1_IMG);
#pragma unroll
  for (int i = 0; i < 5; ++i) {
    u32x2 o;
#pragma unroll
    for (int k = 0; k < 2; ++k) {
      const f32x2 d0 = f[2 * i + k] - mb;
      const u32 d = pk_bf16(d0[0], d0[1]);
      const f32x2 n0 = f32x2{bf_lo(d), bf_hi(d)} * inv;
      const u32 n = pk_bf16(n0[0], n0[1]);
      o[k] = pk_f16(bf_lo(n), bf_hi(n));
    }
    *(u32x2*)(img + h * K1_IMG + 8 * (r + 32 * i)) = o;
  }
  if (r < 8) { u32x2 z = {0u, 0u}; *(u32x2*)(img + h * K1_IMG + 1280 + 8 * r) = z; }
  asm volatile("" ::: "memory");
  STAMP(X1 - X1_OFF, 1, 2);
  const int q4 = (lane & 15) >> 2, p4 = lane & 3, blk = (lane >> 4) & 1;
  const char* tb = img + (4 * h + q4) * 40 + 32 * blk + 8 * p4;
  f32x16 Dr = zero16(), Di = zero16();
#pragma unroll
  for (int s = 0; s < 2; ++s) {
    u32x2 r0 = tr_read(tb + 640 * s), r1 = tr_read(tb + 640 * s + 320);
    u32x2 i0 = tr_read(tb + K1_IMG + 640 * s), i1 = tr_read(tb + K1_IMG + 640 * s + 320);
    u32x4 ar = {r0[0], r0[1], r1[0], r1[1]}, ai = {i0[0], i0[1], i1[0], i1[1]};
    Dr = mf16(ar, C1[s], Dr); Di = mf16(ai, C1[s], Di);
    Dr = mf16(ai, S1[s], Dr); Di = mf16(ar, N1[s], Di);
  }
  u32 er[6], ei[6];
#pragma unroll
  for (int k = 0; k < 6; ++k) {
    const f32x2 dr = lo2(Dr, 2 * k), di = lo2(Di, 2 * k);
    er[k] = pk_f16v(dr * tc[k] + di * ts[k]);
    ei[k] = pk_f16v(di * tc[k] - dr * ts[k]);
  }
  const u32x4 pr = {er[0], er[1], er[2], er[3]}, pi = {ei[0], ei[1], ei[2], ei[3]}, pm = {er[4], er[5], ei[4], ei[5]};
  f32x16 Xr = zero16(), Xi = zero16();
  Xr = mf16(C2, pr, Xr); Xi = mf16(N2, pr, Xi);
  Xr = mf16(S2, pi, Xr); Xi = mf16(C2, pi, Xi);
  Xr = mf16(MR, pm, Xr); Xi = mf16(MI, pm, Xi);
  char* planes = smem + K1_PL0;
  u32* pl = (u32*)(planes + w * K1_PLANE) + 33 * 4 * h + r;
#pragma unroll
  for (int i = 0; i < 8; ++i) pl[33 * crow(i, 0)] = pk_f16(Xr[i], Xi[i]);
  if (h == 0) {
#pragma unroll
    for (int i = 8; i < 12; ++i) pl[33 * crow(i, 0)] = pk_f16(Xr[i], Xi[i]);
  }
  STAMP(X1 - X1_OFF, 1, 3);
  asm volatile("s_waitcnt vmcnt(0) lgkmcnt(0)" : "+v"(e[0]), "+v"(e[1]), "+v"(e[2])::"memory");
  raw_bar();
  STAMP(X1 - X1_OFF, 1, 4);
  u32x4* dst = (u32x4*)(X1 + (size_t)(wg >> 1) * (NSLOT * 32)) + (aq & 1) * 4 + (tid >> 2) * 8 + (tid & 3);
#pragma unroll
  for (int it = 0; it < 3; ++it) {
    if (it < 2 || tid < NSLOT - 512) {
      const u32* sp = (const u32*)planes + (e[it] & 0xffffu);
      const u32 cj = e[it] & 0x80000000u;
      u32x4 o = {sp[0] ^ cj, sp[K1_PLANE / 4] ^ cj, sp[2 * (K1_PLANE / 4)] ^ cj, sp[3 * (K1_PLANE / 4)] ^ cj};
      dst[512 * it] = o;
    }
  }
}

DI int wt_off(int i, int h, int a) { return (((a >> 4) * 4 + (i >> 1)) * 64 + h * 32 + (i & 1) * 16 + (a & 15)) * 16; }

DI void k1_wt(const u16* __restrict__ wr, const u16* __restrict__ wi, char* __restrict__ WT, char* smem, int wg) {
  const int tid = threadIdx.x, lane = tid & 63, w = wave_id();
  const int qh = wg & 1, ao = (wg >> 1) & 3, h = (wg >> 3) & 1, i = wg >> 4;
  const int p0 = 8 * ao + 32 * (crow(2 * i, 0) + 4 * h), q0 = 161 * qh, nq = 161 - qh;
  {
    const char* src = (const char*)(w >> 1 ? wi : wr);
    const u32 lds = lds_addr(smem) + (w >> 1) * 11264;
#pragma unroll
    for (int n = 0; n < 6; ++n) {
      const int ins = (w & 1) * 6 + n;
      if (ins < 11) {
        const int e = ins * 64 + lane, row = e / 22, c = e - 22 * row;
        const int p = p0 + (row & 7) + ((row >> 3) & 1) * 1024 + (row >> 4) * 32;
        const u32 vo = min((u32)((642 * p + 2 * q0) & ~15) + 16u * c, 2048u * 642u - 16u);
        dma<1>(vo, src, lds + ins * 1024);
      }
    }
  }
  wait_vm<0>();
  raw_bar();
  const int alo = tid & 7, a = 8 * ao + alo;
  const char* lb = smem + alo * 352 + 2 * ((alo + q0) & 7) + 2 * (tid >> 3);
  char* ob = WT + (size_t)(q0 + (tid >> 3)) * 8192 + wt_off(i, h, a);
#pragma unroll
  for (int it = 0; it < 6; ++it) {
    const int ql = (tid >> 3) + 32 * it;
    if (ql < nq) {
      u32x4 o;
#pragma unroll
      for (int d = 0; d < 4; ++d)
        o[d] = (u32) * (const u16*)(lb + 64 * it + d * 2816) | ((u32) * (const u16*)(lb + 64 * it + d * 2816 + 11264) << 16);
      *(u32x4*)(ob + (size_t)it * (32 * 8192)) = o;
      if (it == 0 || it == 4) {
        const int q = q0 + ql;
        if (q == 0 || q == 320) {
          char* mb = WT + (size_t)(q ? 322 : 321) * 8192;
#pragma unroll
          for (int d = 0; d < 4; ++d) {
            const int k = p0 + alo + (d & 1) * 1024 + (d >> 1) * 32, kp = (2048 - k) & 2047;
            const int pp = kp & 1023, half = kp >> 10, a2 = pp & 31, bb = pp >> 5;
            const int e = (bb & 3) + 4 * (bb >> 3);
            *(u32*)(mb + wt_off(e >> 1, (bb >> 2) & 1, a2) + (e & 1) * 8 + half * 4) = o[d];
          }
        }
      }
    }
  }
}

__global__ __launch_bounds__(256, 2) void k1_kernel(const u16* __restrict__ x, const u16* __restrict__ wr,
                                                    const u16* __restrict__ wi, char* __restrict__ ws) {
  __shared__ __attribute__((aligned(16))) char smem[K1_LDS];
  const int bid = blockIdx.x;
  STAMP(ws, 1, 0);
  if (bid < 256) k1_row(x, ws + X1_OFF, smem, (bid & 7) * 32 + (bid >> 3));
  else k1_wt(wr, wi, ws + WT_OFF, smem, bid - 256);
  STAMP(ws, 1, 13);
  STAMPE(ws, 1);
}

constexpr int K2_W1_PITCH = 288, K2_W1_MAT = 80 * K2_W1_PITCH + 128;
constexpr int K2_X = 0, K2_ST = 0, K2_W = 16384, K2_TH = 32768, K2_E1 = 40960, K2_E2 = 49152, K2_EB = 57344;
constexpr int K2_LDS = K2_EB + 64;
constexpr int K2_GRID = 168 + 128;

DI void k2_col(const char* __restrict__ X1, const char* __restrict__ WT, char* __restrict__ X2, unsigned char* __restrict__ ecol,
               char* smem, int wg) {
  const int tid = threadIdx.x, lane = tid & 63, w = wave_id(), r = lane & 31, h = lane >> 5;
  const int pw = w & 1, sd = w >> 1, rho = (lane >> 4) & 1;
  const u32 lds0 = lds_addr(smem);
  const u32 l16 = lane * 16;
  {
    const char* xb = X1 + (size_t)wg * 128 + (size_t)w * (NSLOT * 32);
#pragma unroll
    for (int n = 0; n < 4; ++n) {
      const int rr = n * 8 + (lane >> 3);
      const u32 vo = (u32)rr * (u32)(4 * NSLOT * 32) + (u32)(((lane & 7) ^ ((rr >> 1) & 7)) * 16);
      dma<1>(vo, xb, lds0 + K2_X + (w * 4 + n) * 1024);
    }
  }
  const ColTask tk = col_task(wg, pw);
  const int a1 = tk.a1, b1 = tk.b1, am = tk.am, bm = tk.bm;
  const bool valid = tk.valid;
  const bool direct = wg >= 155, self = wg == 155 || wg == 156;
  const int q1 = a1 + 32 * b1, qm = am + 32 * bm;
  const bool swp = q1 > 320;
  const int task = (self && pw) ? (wg == 155 ? 321 : 322) : (swp ? qm : q1);
  dma<4>(l16, WT + (size_t)task * 8192 + sd * 4096, lds0 + K2_W + (pw * 2 + sd) * 4096);
  dma<2>(l16, (const char*)&g_theta + w * 2048, lds0 + K2_TH + w * 2048);
  u32x4 BR[4], BI[4], Cp[2], Sp[2], Np[2];
  f32x2 tc[8], ts[8];
  {
    float c[16], s[16];
    dft32_consts(c, s, r, h);
    pack_csn(c, s, Cp, Sp, Np);
#pragma unroll
    for (int i = 0; i < 16; ++i) { BR[i >> 2][i & 3] = pk_f16(c[i], s[i]); BI[i >> 2][i & 3] = pk_f16(-s[i], c[i]); }
    const float rf = (float)r * (1.f / 1024.f), r4 = rf * (float)(4 * h);
#pragma unroll
    for (int i = 0; i < 16; ++i) {
      const float rev = rf * (float)crow(i, 0) + r4;
      tc[i >> 1][i & 1] = cosr(rev); ts[i >> 1][i & 1] = sinr(rev);
    }
#pragma unroll
    for (int i = 0; i < 4; ++i) { pin(BR[i]); pin(BI[i]); }
#pragma unroll
    for (int i = 0; i < 2; ++i) { pin(Cp[i]); pin(Sp[i]); pin(Np[i]); }
#pragma unroll
    for (int i = 0; i < 8; ++i) { pin(tc[i]); pin(ts[i]); }
  }
  STAMPN(X2 - X2_OFF, 2, 9);
  wait_vm<6>();
  raw_bar();
  STAMPF(X2 - X2_OFF, 2, 9);
  STAMP(X2 - X2_OFF, 2, 1);
  const int slot = (((sd == 1) != swp) ? 2 : 0) + pw;
  f32x16 Zr = zero16(), Zi = zero16();
  {
    const char* xr = smem + K2_X + r * 128 + (((h * 4 + slot) ^ ((r >> 1) & 7)) * 16);
#pragma unroll
    for (int ks = 0; ks < 4; ++ks) {
      const u32x4 a = *(const u32x4*)(xr + ks * 4096);
      Zr = mf16(a, BR[ks], Zr); Zi = mf16(a, BI[ks], Zi);
    }
  }
  STAMP(X2 - X2_OFF, 2, 2);
  u32x4 P[4];
#pragma unroll
  for (int k = 0; k < 8; ++k) {
    const f32x2 dr = lo2(Zr, 2 * k), di = lo2(Zi, 2 * k);
    P[k >> 2][k & 3] = pk_f16v(dr * tc[k] + di * ts[k]);
    P[2 + (k >> 2)][k & 3] = pk_f16v(di * tc[k] - dr * ts[k]);
  }
  {
    u32x4* e1w = (u32x4*)(smem + K2_E1 + w * 2048) + (h * 16 + (lane & 15));
    const u32x4* e1r = (const u32x4*)(smem + K2_E1 + (w ^ 2) * 2048) + (h * 16 + (lane & 15));
    if (rho != sd) {
#pragma unroll
      for (int f = 0; f < 4; ++f) e1w[32 * f] = P[f];
    }
    wait_vm<0>();
    wait_lgkm0();
    raw_bar();
    if (rho != sd) {
#pragma unroll
      for (int f = 0; f < 4; ++f) P[f] = e1r[32 * f];
    }
  }
  STAMP(X2 - X2_OFF, 2, 3);
  Zr = zero16(); Zi = zero16();
#pragma unroll
  for (int s = 0; s < 2; ++s) {
    Zr = mf16(Cp[s], P[s], Zr); Zi = mf16(Cp[s], P[2 + s], Zi);
    Zr = mf16(Sp[s], P[2 + s], Zr); Zi = mf16(Np[s], P[s], Zi);
  }
  f32x2 alr[4], ali[4], ber[4], bei[4], dcr[4], dci[4];
  {
    const u32x4* wl = (const u32x4*)(smem + K2_W + (pw * 2 + sd) * 4096) + lane;
    const u32x4* wsl = (const u32x4*)(smem + K2_W + (2 + sd) * 4096) + lane;
    const f32x4* th = (const f32x4*)(smem + K2_TH + sd * 4096) + lane;
#pragma unroll
    for (int g = 0; g < 4; ++g) {
      const u32x4 t = wl[64 * g];
      f32x2 w0r = {bf_lo(t[0]), bf_lo(t[2])}, w0i = {bf_hi(t[0]), bf_hi(t[2])};
      f32x2 w1r = {bf_lo(t[1]), bf_lo(t[3])}, w1i = {bf_hi(t[1]), bf_hi(t[3])};
      if (self) {
        const u32x4 u = wsl[64 * g];
        w0r = (w0r + f32x2{bf_lo(u[0]), bf_lo(u[2])}) * 0.5f; w0i = (w0i - f32x2{bf_hi(u[0]), bf_hi(u[2])}) * 0.5f;
        w1r = (w1r + f32x2{bf_lo(u[1]), bf_lo(u[3])}) * 0.5f; w1i = (w1i - f32x2{bf_hi(u[1]), bf_hi(u[3])}) * 0.5f;
      }
      const f32x4 thc = th[64 * (g >> 1)], ths = th[64 * (2 + (g >> 1))];
      const f32x2 ct = pr2(thc, 2 * (g & 1)), st = pr2(ths, 2 * (g & 1));
      const f32x2 sr = w0r + w1r, si = w0i + w1i, dr = w0r - w1r, di = w0i - w1i;
      alr[g] = sr - dr * st; ali[g] = si - di * st; ber[g] = sr + dr * st; bei[g] = si + di * st;
      dcr[g] = dr * ct; dci[g] = di * ct;
    }
  }
  STAMP(X2 - X2_OFF, 2, 4);
  f32x2 nur[4], nui[4], nvr[4], nvi[4];
  float mx = 0.f;
#pragma unroll
  for (int g = 0; g < 4; ++g) {
    f32x2 ur, ui, vr, vi;
#pragma unroll
    for (int k = 0; k < 2; ++k) {
      auto sr_ = __builtin_amdgcn_permlane16_swap(asu(Zr[4 * g + k]), asu(Zr[4 * g + 2 + k]), false, false);
      auto si_ = __builtin_amdgcn_permlane16_swap(asu(Zi[4 * g + k]), asu(Zi[4 * g + 2 + k]), false, false);
      ur[k] = asf(sr_[0]); vr[k] = asf(sr_[1]); ui[k] = asf(si_[0]); vi[k] = asf(si_[1]);
    }
    nur[g] = alr[g] * ur - ali[g] * ui - dcr[g] * vi - dci[g] * vr;
    nui[g] = alr[g] * ui + ali[g] * ur + dcr[g] * vr - dci[g] * vi;
    nvr[g] = ber[g] * vr - bei[g] * vi + dcr[g] * ui + dci[g] * ur;
    nvi[g] = ber[g] * vi + bei[g] * vr - dcr[g] * ur + dci[g] * ui;
#pragma unroll
    for (int k = 0; k < 2; ++k)
      mx = fmaxf(fmaxf(mx, fmaxf(fabsf(nur[g][k]), fabsf(nui[g][k]))), fmaxf(fabsf(nvr[g][k]), fabsf(nvi[g][k])));
  }
  const int eb = (int)umax(max64(asu(mx)) >> 23, 4u);
  const float sc = asf((u32)(258 - eb) << 23);
#pragma unroll
  for (int g = 0; g < 4; ++g) {
    auto qr = __builtin_amdgcn_permlane16_swap(pk_f16v(nur[g] * sc), pk_f16v(nvr[g] * sc), false, false);
    auto qi = __builtin_amdgcn_permlane16_swap(pk_f16v(nui[g] * sc), pk_f16v(nvi[g] * sc), false, false);
    P[g >> 1][2 * (g & 1)] = qr[0]; P[g >> 1][2 * (g & 1) + 1] = qr[1];
    P[2 + (g >> 1)][2 * (g & 1)] = qi[0]; P[2 + (g >> 1)][2 * (g & 1) + 1] = qi[1];
  }
  STAMP(X2 - X2_OFF, 2, 5);
  Zr = zero16(); Zi = zero16();
#pragma unroll
  for (int s = 0; s < 2; ++s) {
    Zr = mf16(P[s], Cp[s], Zr); Zi = mf16(P[s], Sp[s], Zi);
    Zr = mf16(P[2 + s], Np[s], Zr); Zi = mf16(P[2 + s], Cp[s], Zi);
  }
  int em;
  auto finish = [&](auto SD) {
    constexpr int S = decltype(SD)::v, KO = 8 * S, SO = 8 - KO;
    u32x4 kr, ki, orr, oi;
    u32x4* e2w = (u32x4*)(smem + K2_E2 + w * 2048) + lane;
    const u32x4* e2r = (const u32x4*)(smem + K2_E2 + (w ^ 2) * 2048) + lane;
    int* ebx = (int*)(smem + K2_EB);
    if (lane == 0) ebx[w] = eb;
#pragma unroll
    for (int k = 0; k < 4; ++k) {
      const f32x2 dr = lo2(Zr, SO + 2 * k), di = lo2(Zi, SO + 2 * k), c = tc[4 * S + k], sn = ts[4 * S + k];
      orr[k] = pk_f16v(dr * c - di * sn); oi[k] = pk_f16v(di * c + dr * sn);
    }
    e2w[0] = orr; e2w[64] = oi;
#pragma unroll
    for (int k = 0; k < 4; ++k) {
      const f32x2 dr = lo2(Zr, KO + 2 * k), di = lo2(Zi, KO + 2 * k), c = tc[4 * S + k], sn = ts[4 * S + k];
      kr[k] = pk_f16v(dr * c - di * sn); ki[k] = pk_f16v(di * c + dr * sn);
    }
    wait_lgkm0();
    raw_bar();
    orr = e2r[0]; oi = e2r[64];
    const int eo = __builtin_amdgcn_readfirstlane(ebx[w ^ 2]);
    const int ebu = __builtin_amdgcn_readfirstlane(eb);
    em = max(ebu, eo);
    if (ebu != eo) {
      const u32 fk = pow2_h2(ebu - em), fo = pow2_h2(eo - em);
#pragma unroll
      for (int i = 0; i < 4; ++i) { kr[i] = pkmul_h(kr[i], fk); ki[i] = pkmul_h(ki[i], fk); orr[i] = pkmul_h(orr[i], fo); oi[i] = pkmul_h(oi[i], fo); }
    }
    Zr = zero16(); Zi = zero16();
    Zr = mf16(Cp[S], kr, Zr); Zi = mf16(Sp[S], kr, Zi);
    Zr = mf16(Np[S], ki, Zr); Zi = mf16(Cp[S], ki, Zi);
    Zr = mf16(Cp[1 - S], orr, Zr); Zi = mf16(Sp[1 - S], orr, Zi);
    Zr = mf16(Np[1 - S], oi, Zr); Zi = mf16(Cp[1 - S], oi, Zi);
  };
  if (sd) finish(IC<1>{}); else finish(IC<0>{});
  const bool prim = (sd == 1) == swp;
  const int ao = prim ? a1 : am, bo = prim ? b1 : bm;
  const u32 cj = sd ? 0x80000000u : 0u;
  if (valid && lane == 0) ecol[ao * 20 + bo] = (unsigned char)em;
  STAMP(X2 - X2_OFF, 2, 7);
  if (!direct) {
    u32x4* su = (u32x4*)(smem + K2_ST) + (((prim ? 0 : 2) + (bo & 1)) * 4) * 64 + lane;
#pragma unroll
    for (int g = 0; g < 4; ++g) {
      u32x4 o = {pk_f16(Zr[4 * g], Zi[4 * g]) ^ cj, pk_f16(Zr[4 * g + 1], Zi[4 * g + 1]) ^ cj,
                 pk_f16(Zr[4 * g + 2], Zi[4 * g + 2]) ^ cj, pk_f16(Zr[4 * g + 3], Zi[4 * g + 3]) ^ cj};
      su[64 * g] = o;
    }
    wait_lgkm0();
    raw_bar();
    STAMP(X2 - X2_OFF, 2, 8);
    const int c2 = b1 >> 1;
#pragma unroll
    for (int set = 0; set < 2; ++set) {
      const int duo = set ? (am * 10 + 9 - c2) : (a1 * 10 + c2);
      const u32x4* sp = (const u32x4*)(smem + K2_ST) + (set * 8 + w) * 64 + lane;
      const u32x4 p0 = sp[0], p1 = sp[256];
      u32x2* dp = (u32x2*)(X2 + (size_t)(duo >> 1) * 16384) + (duo & 1) + 2 * (32 * (8 * w + 4 * h) + r);
#pragma unroll
      for (int i = 0; i < 4; ++i) { u32x2 o = {p0[i], p1[i]}; dp[64 * i] = o; }
    }
  } else if (valid && !(self && sd)) {
    u32* du = (u32*)(X2 + (size_t)(bo >> 2) * 16384) + (bo & 3) + 4 * (128 * h + r);
#pragma unroll
    for (int i = 0; i < 16; ++i) du[128 * crow(i, 0)] = pk_f16(Zr[i], Zi[i]) ^ cj;
  }
}

DI void k2_w1(const u16* __restrict__ wg_, const u16* __restrict__ wu_, char* __restrict__ W1, char* smem, int unit) {
  const int tid = threadIdx.x, lane = tid & 63, w = wave_id();
  const int t = unit >> 3, u = unit & 7;
  u32x2 v[17];
#pragma unroll
  for (int it = 0; it < 17; ++it) {
    const int e = min(tid + 256 * it, 4319);
    const int row2 = e / 27, c8 = e - 27 * row2, mat = row2 >= 80, row = row2 - 80 * mat;
    const u16* base = mat ? wu_ : wg_;
    v[it] = *(const u32x2*)(base + (size_t)(80 * u + row) * 1728 + 108 * t + 4 * c8);
  }
#pragma unroll
  for (int it = 0; it < 17; ++it) {
    const int e = min(tid + 256 * it, 4319);
    const int row2 = e / 27, c8 = e - 27 * row2, mat = row2 >= 80, row = row2 - 80 * mat;
    *(u32x2*)(smem + mat * K2_W1_MAT + row * K2_W1_PITCH + 8 * c8) = v[it];
  }
  if (tid < 160) { u32x2 z = {0u, 0u}; *(u32x2*)(smem + (tid >= 80) * K2_W1_MAT + (tid % 80) * K2_W1_PITCH + 216) = z; }
  __syncthreads();
  const int h2_ = lane >> 5, blk = (lane >> 4) & 1, q4 = (lane & 15) >> 2, p4 = lane & 3;
  const char* lb = smem + blk * K2_W1_MAT + 8 * p4;
  for (int fr = w; fr < 35; fr += 4) {
    const int kk = fr / 7, ft = fr - 7 * kk;
    const int k16 = kk < 4 ? 4 * u + kk : 32 + u;
    const int r0 = kk < 4 ? 20 * kk + 4 * h2_ + q4 : 40 * h2_ + 16 + q4;
    const int r1 = kk < 4 ? r0 + 8 : r0 + 20;
    u32x2 lo = tr_read(lb + r0 * K2_W1_PITCH + 32 * ft), hi = tr_read(lb + r1 * K2_W1_PITCH + 32 * ft);
    const int pos = ft < 4 ? k16 * 4 + ft : (ft < 6 ? 160 + k16 * 2 + (ft - 4) : 240 + k16);
    u32x4 o = {lo[0], lo[1], hi[0], hi[1]};
    *((u32x4*)(W1 + ((size_t)t * 280 + pos) * 1024) + lane) = o;
  }
}
static_assert(2 * K2_W1_MAT <= K2_LDS, "LDS");

__global__ __launch_bounds__(256, 2) void k2_kernel(const u16* __restrict__ wg_, const u16* __restrict__ wu_, char* __restrict__ ws) {
  __shared__ __attribute__((aligned(16))) char smem[K2_LDS];
  const int bid = blockIdx.x;
  STAMP(ws, 2, 0);
  if (bid < 168) {
    const int lw = (bid & 7) * 21 + (bid >> 3);
    if (lw < NCOLWG) k2_col(ws + X1_OFF, ws + WT_OFF, ws + X2_OFF, (unsigned char*)(ws + EC_OFF), smem, lw);
  } else k2_w1(wg_, wu_, ws + W1_OFF, smem, bid - 168);
  STAMP(ws, 2, 13);
  STAMPE(ws, 2);
}

constexpr int K3_W2_PITCH = 672;
constexpr int K3_X2 = 0, K3_EC = 10240, K3_ST = 12288;
constexpr int K3_LDS = K3_ST + 80 * 144;
constexpr int K3_GRID = 256 + 112;
static_assert(32 * K3_W2_PITCH <= K3_LDS, "LDS");

DI void k3_row(const char* __restrict__ X2, const u16* __restrict__ x, u16* __restrict__ H, char* __restrict__ ACT,
               char* smem, int wg) {
  const int tid = threadIdx.x, lane = tid & 63, w = wave_id(), r = lane & 31, h = lane >> 5;
  const int m = 4 * wg + w;
  const u32 lds0 = lds_addr(smem);
#pragma unroll
  for (int n = 0; n < 3; ++n) {
    const int pc = w + 4 * n;
    const u32 vx = (u32)(pc * 16 + (lane >> 2)) * 16384u + (u32)(wg * 64 + (lane & 3) * 16);
    const u32 ve = (u32)X2_SZ + (pc == 10 ? (u32)min(lane, 39) * 16u : 0u);
    dma<1>(pc < 10 ? vx : ve, X2, lds0 + K3_X2 + pc * 1024);
  }
  u32x2 xr[6];
  {
    const char* src = (const char*)x + (size_t)m * 2560;
    const u32 vo = 40 * r + 8 * h, v2 = 40 * r + 32;
    asm volatile("global_load_dwordx2 %0, %6, %8\n\tglobal_load_dwordx2 %1, %6, %8 offset:16\n\tglobal_load_dwordx2 %2, %7, %8\n\t"
                 "global_load_dwordx2 %3, %6, %8 offset:1280\n\tglobal_load_dwordx2 %4, %6, %8 offset:1296\n\t"
                 "global_load_dwordx2 %5, %7, %8 offset:1280"
                 : "=&v"(xr[0]), "=&v"(xr[1]), "=&v"(xr[2]), "=&v"(xr[3]), "=&v"(xr[4]), "=&v"(xr[5])
                 : "v"(vo), "v"(v2), "s"(src) : "memory");
  }
  u32x4 BR[3], BI[3], Cp[2], Sp[2], Np[2];
  f32x2 tc[8], ts[8];
  {
    float c[16], s[16];
    dft32_consts(c, s, r, h);
    pack_csn(c, s, Cp, Sp, Np);
    const float rf = (float)r * (1.f / 640.f), r4 = rf * (float)(4 * h);
#pragma unroll
    for (int i = 0; i < 16; ++i) {
      const float rev = rf * (float)crow(i, 0) + r4;
      tc[i >> 1][i & 1] = cosr(rev); ts[i >> 1][i & 1] = sinr(rev);
      if (i < 12) {
        const float rev2 = rev * 32.f, c2 = cosr(rev2), s2 = sinr(rev2);
        const u32 br = pk_f16(c2, -s2), bi = pk_f16(s2, c2);
        BR[i >> 2][i & 3] = (i >= 8 && h) ? 0u : br; BI[i >> 2][i & 3] = (i >= 8 && h) ? 0u : bi;
      }
    }
#pragma unroll
    for (int i = 0; i < 3; ++i) { pin(BR[i]); pin(BI[i]); }
#pragma unroll
    for (int i = 0; i < 2; ++i) { pin(Cp[i]); pin(Sp[i]); pin(Np[i]); }
#pragma unroll
    for (int i = 0; i < 8; ++i) { pin(tc[i]); pin(ts[i]); }
  }
  STAMPN(ACT - ACT_OFF, 3, 9);
  wait_vm<6>();
  raw_bar();
  STAMPF(ACT - ACT_OFF, 3, 9);
  STAMP(ACT - ACT_OFF, 3, 1);
  u32x4 a[3];
  u32 ec[3];
#pragma unroll
  for (int ks = 0; ks < 3; ++ks) {
    const int c = (ks == 2) ? 4 : 2 * ks + h;
    a[ks] = *(const u32x4*)(smem + K3_X2 + (r * 5 + c) * 64 + w * 16);
    ec[ks] = *(const u32*)(smem + K3_EC + (r * 5 + c) * 4);
  }
  int ex[12];
  u32 em = 0;
#pragma unroll
  for (int i = 0; i < 12; ++i) { ex[i] = (ec[i >> 2] >> (8 * (i & 3))) & 0xff; em = umax(em, (u32)ex[i]); }
  const int E = (int)max64(em);
#pragma unroll
  for (int i = 0; i < 12; ++i) a[i >> 2][i & 3] = pkmul_h(a[i >> 2][i & 3], pow2_h2(ex[i] - (E + 5)));
  const float fs = __builtin_amdgcn_ldexpf(1.f / 1280.f, E - 127 - 9);
  STAMP(ACT - ACT_OFF, 3, 2);
  f32x16 Tr = zero16(), Ti = zero16();
#pragma unroll
  for (int ks = 0; ks < 3; ++ks) { Tr = mf16(a[ks], BR[ks], Tr); Ti = mf16(a[ks], BI[ks], Ti); }
  asm volatile("s_waitcnt vmcnt(0)" : "+v"(xr[0]), "+v"(xr[1]), "+v"(xr[2]), "+v"(xr[3]), "+v"(xr[4]), "+v"(xr[5])::"memory");
  touch35k(ACT - ACT_OFF + W1_OFF + (size_t)(wg >> 6) * (1120 * 1024) + (size_t)(wg & 31) * (35 * 1024), tid, lds0 + K3_EC + 1024);
  u32x4 P[4];
#pragma unroll
  for (int k = 0; k < 8; ++k) {
    const f32x2 dr = lo2(Tr, 2 * k), di = lo2(Ti, 2 * k);
    P[k >> 2][k & 3] = pk_f16v(dr * tc[k] - di * ts[k]);
    P[2 + (k >> 2)][k & 3] = pk_f16v(di * tc[k] + dr * ts[k]);
  }
  f32x16 Vr = zero16(), Vi = zero16();
#pragma unroll
  for (int s = 0; s < 2; ++s) {
    Vr = mf16(P[s], Cp[s], Vr); Vi = mf16(P[s], Sp[s], Vi);
    Vr = mf16(P[2 + s], Np[s], Vr); Vi = mf16(P[2 + s], Cp[s], Vi);
  }
  STAMP(ACT - ACT_OFF, 3, 3);
  f32x2 hv[2][6];
  u32 hp[2][6];
  float mean[2], var[2];
#pragma unroll
  for (int rr = 0; rr < 2; ++rr) {
    f32x2 sm = {0.f, 0.f};
#pragma unroll
    for (int k = 0; k < 6; ++k) {
      const f32x2 y = (rr ? lo2(Vi, 2 * k) : lo2(Vr, 2 * k)) * fs;
      const u32 yb = pk_bf16(y[0], y[1]);
      const u32 xx = xr[3 * rr + (k >> 1)][k & 1];
      const f32x2 hs = f32x2{bf_lo(xx), bf_hi(xx)} + f32x2{bf_lo(yb), bf_hi(yb)};
      u32 hh = pk_bf16(hs[0], hs[1]);
      if (k >= 4) hh = h ? 0u : hh;
      hp[rr][k] = hh;
      hv[rr][k] = f32x2{bf_lo(hh), bf_hi(hh)};
      sm += hv[rr][k];
    }
    mean[rr] = sm[0] + sm[1];
  }
#pragma unroll
  for (int rr = 0; rr < 2; ++rr) mean[rr] = sum64(mean[rr]) * (1.f / 640.f);
#pragma unroll
  for (int rr = 0; rr < 2; ++rr) {
    f32x2 q2 = {0.f, 0.f}, q3 = {0.f, 0.f};
#pragma unroll
    for (int k = 0; k < 4; ++k) { const f32x2 d = hv[rr][k] - mean[rr]; q2 += d * d; }
#pragma unroll
    for (int k = 4; k < 6; ++k) { const f32x2 d = hv[rr][k] - mean[rr]; q3 += d * d; }
    var[rr] = q2[0] + q2[1] + (h ? 0.f : q3[0] + q3[1]);
  }
#pragma unroll
  for (int rr = 0; rr < 2; ++rr) var[rr] = sum64(var[rr]) * (1.f / 640.f);
#pragma unroll
  for (int rr = 0; rr < 2; ++rr) {
    float mb, inv;
    ln_scalars(mean[rr], var[rr], mb, inv);
    u32 o[6];
#pragma unroll
    for (int k = 0; k < 6; ++k) {
      const f32x2 d0 = hv[rr][k] - mb;
      const u32 d = pk_bf16(d0[0], d0[1]);
      const f32x2 n0 = f32x2{bf_lo(d), bf_hi(d)} * inv;
      o[k] = pk_bf16(n0[0], n0[1]);
    }
    const int rowl = 2 * w + rr;
    u32x4 o4 = {o[0], o[1], o[2], o[3]};
    *(u32x4*)(smem + K3_ST + (h * 32 + r) * 144 + rowl * 16) = o4;
    if (h == 0) { u32x2 o2 = {o[4], o[5]}; *(u32x2*)(smem + K3_ST + (64 + (r >> 1)) * 144 + rowl * 16 + (r & 1) * 8) = o2; }
  }
  STAMP(ACT - ACT_OFF, 3, 4);
  wait_lgkm0();
  raw_bar();
  STAMP(ACT - ACT_OFF, 3, 5);
  const int row0 = 8 * wg;
  char* ab = ACT + (size_t)(row0 >> 5) * (40 * 1024) + ((row0 & 31) + (tid & 7)) * 16;
#pragma unroll
  for (int it = 0; it < 3; ++it) {
    const int gi = (tid >> 3) + 32 * it;
    if (gi < 80) {
      const int L = gi < 64 ? (gi & 1) * 32 + (gi >> 1) : gi;
      u32x4 o = *(const u32x4*)(smem + K3_ST + L * 144 + (tid & 7) * 16);
      *(u32x4*)(ab + gi * 512) = o;
    }
  }
#pragma unroll
  for (int rr = 0; rr < 2; ++rr) {
    u16* hrow = H + (size_t)(2 * m + rr) * 640 + 20 * r;
    u32x2 o0 = {hp[rr][0], hp[rr][1]}, o1 = {hp[rr][2], hp[rr][3]}, o2 = {hp[rr][4], hp[rr][5]};
    *(u32x2*)(hrow + 4 * h) = o0;
    *(u32x2*)(hrow + 8 + 4 * h) = o1;
    if (h == 0) *(u32x2*)(hrow + 16) = o2;
  }
}

DI void k3_w2(const u16* __restrict__ wd, char* __restrict__ W2, char* smem, int idx) {
  const int tid = threadIdx.x, lane = tid & 63, w = wave_id();
  const int k32 = idx >> 1, nh = idx & 1;
  u32x4 v[5];
#pragma unroll
  for (int it = 0; it < 5; ++it) {
    const int e = tid + 256 * it;
    const int row = e / 40, c = e - 40 * row;
    const int u2 = 2 * k32 + (row >> 4), t = u2 / 7, ft = u2 - 7 * t, fl = 16 * ft + (row & 15);
    u32x4 z = {0u, 0u, 0u, 0u};
    v[it] = fl < 108 ? *(const u32x4*)(wd + (size_t)(108 * t + fl) * 640 + 320 * nh + 8 * c) : z;
  }
#pragma unroll
  for (int it = 0; it < 5; ++it) {
    const int e = tid + 256 * it;
    const int row = e / 40, c = e - 40 * row;
    *(u32x4*)(smem + row * K3_W2_PITCH + 16 * c) = v[it];
  }
  __syncthreads();
  const int g = lane >> 4, q4 = (lane & 15) >> 2, p4 = lane & 3;
  const char* lb = smem + (16 * (g >> 1) + 4 * (g & 1) + q4) * K3_W2_PITCH + 8 * p4;
#pragma unroll
  for (int i = 0; i < 5; ++i) {
    const int fr = w * 5 + i;
    u32x2 lo = tr_read(lb + 32 * fr), hi = tr_read(lb + 8 * K3_W2_PITCH + 32 * fr);
    u32x4 o = {lo[0], lo[1], hi[0], hi[1]};
    *((u32x4*)(W2 + ((size_t)((4 * nh + w) * 56 + k32) * 5 + i) * 1024) + lane) = o;
  }
}

__global__ __launch_bounds__(256, 2) void k3_kernel(const u16* __restrict__ x, const u16* __restrict__ wd, char* __restrict__ ws) {
  __shared__ __attribute__((aligned(16))) char smem[K3_LDS];
  const int bid = blockIdx.x;
  STAMP(ws, 3, 0);
  if (bid < 256) k3_row(ws + X2_OFF, x, (u16*)(ws + H_OFF), ws + ACT_OFF, smem, (bid & 7) * 32 + (bid >> 3));
  else k3_w2(wd, ws + W2_OFF, smem, bid - 256);
  STAMP(ws, 3, 13);
  STAMPE(ws, 3);
}

constexpr int K4_SLOT = 40 * 1024;
constexpr int K4_P1 = 40;
constexpr int K4_KS = 5;
constexpr int K4_NS1 = 8;
constexpr int K4_NSTAGE = 11;
constexpr int K4_NSTEP = 70;
constexpr int K4_PD = 2;
constexpr int k4_stage_of(int step) { return step < K4_P1 ? step / K4_KS : K4_NS1 + (step - K4_P1) / 10; }
constexpr bool k4_first(int step) { return step < K4_P1 ? step % K4_KS == 0 : (step - K4_P1) % 10 == 0; }
constexpr int k4_loads(int units) { return 10 * (units / 4) + (units % 4 == 0 ? 0 : units % 4 == 1 ? 3 : units % 4 == 2 ? 6 : 8); }
constexpr int k4_units_before(int i) {
  int n = 8;
  for (int k = 0; k < i; ++k) {
    const int synced = k4_stage_of(k + K4_PD < K4_NSTEP ? k + K4_PD : K4_NSTEP - 1);
    const int avail = 4 * (synced + 4 < K4_NSTAGE ? synced + 4 : K4_NSTAGE);
    const int want = n + (k < 4 ? 2 : 1);
    n = want < avail ? want : avail;
  }
  return n;
}

__global__ __launch_bounds__(256, 1) void k4_kernel(char* __restrict__ ws) {
  __shared__ __attribute__((aligned(16))) char smem[4 * K4_SLOT];
  const int tid = threadIdx.x, lane = tid & 63, w = wave_id(), r = lane & 31, h = lane >> 5;
  const int bid = blockIdx.x, xcd = bid & 7, idx = bid >> 3;
  const int rb = 4 * ((idx >> 1) & 3) + 2 * (xcd & 1) + (idx & 1), t = 4 * (xcd >> 1) + (idx >> 3);
  const char* actg = ws + ACT_OFF + (size_t)(4 * rb + w) * (40 * 1024);
  const char* w1g = ws + W1_OFF + (size_t)t * (280 * 1024);
  const u32 voff = lane * 16;
  const u32 lds0 = lds_addr(smem);

  auto issue = [&](auto U) {
    constexpr int s = decltype(U)::v / 4, p = decltype(U)::v % 4;
    constexpr int n = p < 2 ? 3 : 2;
    const u32 slot = lds0 + (s & 3) * K4_SLOT;
    if constexpr (s < K4_NS1) {
      constexpr int o = (p >> 1) * 3072;
      if constexpr (p & 1) dma<n>(voff, w1g + s * 20480 + w * 5120 + o, slot + 20480 + w * 5120 + o);
      else dma<n>(voff, actg + s * 5120 + o, slot + w * 5120 + o);
    } else {
      constexpr int o = k4_loads(p) * 1024;
      dma<n>(voff, w1g + 160 * 1024 + (s - K4_NS1) * 40960 + w * 10240 + o, slot + w * 10240 + o);
    }
  };
  STAMP(ws, 4, 0);
  static_for<0, 8>(issue);

  u32 lb[4], lab[4];
#pragma unroll
  for (int i = 0; i < 4; ++i) {
    lb[i] = lane * 16 + i * K4_SLOT; lab[i] = lb[i] + w * 5120;
    pin(lb[i]); pin(lab[i]);
  }
  u32x4 act[40];
  u32x4 wf[K4_PD + 1][4];
  f32x16 acc[8];
#pragma unroll
  for (int i = 0; i < 8; ++i) acc[i] = zero16();
  u32 gout[4];
  f32x2 eg, eu, ez;

  auto rdw = [&](auto I, auto C) {
    constexpr int i = decltype(I)::v, c = decltype(C)::v, b = i % (K4_PD + 1);
    if constexpr (i < K4_P1) {
      constexpr int s = i / K4_KS, kl = i % K4_KS;
      wf[b][c] = *(const u32x4*)(smem + lb[s & 3] + (20480 + (kl * 4 + c) * 1024));
    } else if constexpr (i < K4_NSTEP) {
      constexpr int s = K4_NS1 + (i - K4_P1) / 10, u = (i - K4_P1) % 10;
      wf[b][c] = *(const u32x4*)(smem + lb[s & 3] + (u * 4 + c) * 1024);
    }
  };
  auto rda = [&](auto I) {
    constexpr int i = decltype(I)::v;
    if constexpr (i < K4_P1) act[i] = *(const u32x4*)(smem + lab[(i / K4_KS) & 3] + (i % K4_KS) * 1024);
  };
  const int m = 128 * rb + 32 * w + r;
  char* gdst = ws + G_OFF + ((size_t)((m >> 6) * 56) * 4 + ((m >> 4) & 3)) * 1024 + (m & 15) * 16;
  auto epi = [&](auto FT, auto P, auto ST) {
    constexpr int ft = decltype(FT)::v, p = decltype(P)::v, st = decltype(ST)::v;
    if constexpr (st == 0) { eg = lo2(acc[ft], 2 * p); eu = lo2(acc[ft], 8 + 2 * p); ez = eg * eg; }
    if constexpr (st == 1) {
      ez = eg * (ez * -0.1029432397f + -2.3022081983f);
      ez = f32x2{__builtin_amdgcn_exp2f(ez[0]), __builtin_amdgcn_exp2f(ez[1])};
    }
    if constexpr (st == 2) {
      ez = ez + 1.f;
      ez = f32x2{__builtin_amdgcn_rcpf(ez[0]), __builtin_amdgcn_rcpf(ez[1])};
    }
    if constexpr (st == 3) { const f32x2 o = (eg * ez) * eu; gout[p] = pk_bf16(o[0], o[1]); }
  };
  auto epi_store = [&](auto FT) {
    constexpr int ft = decltype(FT)::v;
    const int gam = 14 * t + 2 * ft + h;
    u32x4 o = {gout[0], gout[1], gout[2], gout[3]};
    *(u32x4*)(gdst + (gam >> 2) * 4096 + (gam & 3) * 256) = o;
  };

  wait_vm<10>();
  raw_bar();
  STAMP(ws, 4, 1);
  static_for<0, K4_PD>([&](auto I) { rda(I); static_for<0, 4>([&](auto C) { rdw(I, C); }); });
  constexpr int TOUCH_STEP = 42;
  static_for<0, K4_NSTEP>([&](auto I) {
    constexpr int i = decltype(I)::v;
    constexpr int nx = i + K4_PD, b = i % (K4_PD + 1), u = i - K4_P1;
    constexpr int n0 = k4_units_before(i), n1 = k4_units_before(i + 1);
    static_assert(i < K4_P1 || n1 == n0, "all DMA issued in phase 1");
    static_for<0, 4>([&](auto C) {
      constexpr int c = decltype(C)::v;
      if constexpr (i < K4_P1) acc[c] = mb32(wf[b][c], act[i], acc[c]);
      else if constexpr (u < 20) acc[4 + (c & 1)] = mb32(wf[b][c], act[2 * u + (c >> 1)], acc[4 + (c & 1)]);
      else acc[6 + (c & 1)] = mb32(wf[b][c], act[4 * (u - 20) + c], acc[6 + (c & 1)]);
      __builtin_amdgcn_sched_barrier(0);
      if constexpr (c == 0 && nx < K4_NSTEP) {
        if constexpr (k4_first(nx)) {
          constexpr int sn = k4_stage_of(nx);
          static_assert(n0 >= 4 * (sn + 1), "stage not issued before its wait");
          wait_lgkm0();
          wait_vm<k4_loads(n0) - 10 * (sn + 1) + (i > TOUCH_STEP ? 2 : 0)>();
          raw_bar();
        }
        rda(IC<nx>{});
      }
      rdw(IC<nx>{}, C);
      if constexpr (c == 1 && n1 > n0) issue(IC<n0>{});
      if constexpr (c == 3 && n1 > n0 + 1) issue(IC<n0 + 1>{});
      if constexpr (c == 1 && i == TOUCH_STEP)
        touch35k(ws + W2_OFF + (size_t)((xcd >> 1) & 1) * (1120 * 1024) + (size_t)idx * (35 * 1024), tid, lds0 + 3 * K4_SLOT);
      if constexpr (i >= K4_P1) {
        constexpr int e = u / 5, ph = u % 5;
        if constexpr (ph < 4) epi(IC<e>{}, IC<ph>{}, C);
        else if constexpr (c == 0) epi_store(IC<e>{});
      }
      __builtin_amdgcn_sched_barrier(0);
    });
  });
  STAMP(ws, 4, 8);
  acc[6] += acc[7];
  {
    u32x4 o;
#pragma unroll
    for (int p = 0; p < 4; ++p) {
      const f32x2 g = lo2(acc[6], 2 * p), u = lo2(acc[6], 8 + 2 * p);
      const f32x2 z = g * (g * g * -0.1029432397f + -2.3022081983f);
      const f32x2 den = f32x2{__builtin_amdgcn_exp2f(z[0]), __builtin_amdgcn_exp2f(z[1])} + 1.f;
      const f32x2 ov = (g * f32x2{__builtin_amdgcn_rcpf(den[0]), __builtin_amdgcn_rcpf(den[1])}) * u;
      o[p] = pk_bf16(ov[0], ov[1]);
    }
    const int gam = 14 * t + 12 + h;
    *(u32x4*)(gdst + (gam >> 2) * 4096 + (gam & 3) * 256) = o;
  }
  STAMP(ws, 4, 13);
  STAMPE(ws, 4);
}

constexpr int K5_R = 4, K5_STEP = 9 * 1024, K5_RING = 40 * 1024;
__global__ __launch_bounds__(256, 1) void k5_kernel(const char* __restrict__ ws, u16* __restrict__ out) {
  __shared__ __attribute__((aligned(16))) char smem[4 * K5_RING];
  const int tid = threadIdx.x, lane = tid & 63, w = wave_id();
  const int bid = blockIdx.x, xcd = bid & 7, idx = bid >> 3;
  const int mb = 4 * ((xcd & 1) + 4 * (xcd >> 2) + 2 * ((idx >> 2) & 1)) + (idx & 3), nb = 4 * ((xcd >> 1) & 1) + (idx >> 3);
  const char* gg = ws + G_OFF + (size_t)(mb * 56 + 14 * w) * 4096;
  const char* wg = ws + W2_OFF + (size_t)(nb * 56 + 14 * w) * 5120;
  const u32 voff = lane * 16;
  const u32 ring = lds_addr(smem) + w * K5_RING;
  auto issue = [&](auto S) {
    constexpr int s = decltype(S)::v;
    if constexpr (s < 14) {
      const u32 slot = ring + (s % K5_R) * K5_STEP;
      dma<4>(voff, gg + s * 4096, slot);
      dma<4>(voff, wg + s * 5120, slot + 4096);
      dma<1>(voff, wg + s * 5120 + 4096, slot + 8192);
    }
  };
  STAMP(ws, 5, 0);
  static_for<0, K5_R>(issue);
  const int mrow = 64 * mb + 16 * w + (lane & 15), ncol = 80 * nb + 4 * (lane >> 4);
  const char* lw = smem + w * K5_RING + lane * 16;
  const char* lg[4];
#pragma unroll
  for (int i = 0; i < 4; ++i) lg[i] = lw + ((w + i) & 3) * 1024;
  u32x4 gb[2][4], wb[2][5];
  auto rd = [&](auto S) {
    constexpr int s = decltype(S)::v, so = (s % K5_R) * K5_STEP;
#pragma unroll
    for (int i = 0; i < 5; ++i) wb[s & 1][i] = *(const u32x4*)(lw + so + 4096 + i * 1024);
#pragma unroll
    for (int i = 0; i < 4; ++i) gb[s & 1][i] = *(const u32x4*)(lg[i] + so);
  };
  f32x4 acc[4][5];
#pragma unroll
  for (int i = 0; i < 4; ++i)
#pragma unroll
    for (int j = 0; j < 5; ++j) acc[i][j] = f32x4{0.f, 0.f, 0.f, 0.f};
  u32x2 hv[5];
  {
    const char* hp = ws + H_OFF + ((size_t)mrow * 640 + ncol) * 2;
    asm volatile("global_load_dwordx2 %0, %5, off\n\tglobal_load_dwordx2 %1, %5, off offset:32\n\t"
                 "global_load_dwordx2 %2, %5, off offset:64\n\tglobal_load_dwordx2 %3, %5, off offset:96\n\t"
                 "global_load_dwordx2 %4, %5, off offset:128"
                 : "=&v"(hv[0]), "=&v"(hv[1]), "=&v"(hv[2]), "=&v"(hv[3]), "=&v"(hv[4]) : "v"(hp) : "memory");
  }
  wait_vm<9 * (K5_R - 1) + 5>();
  rd(IC<0>{});
  wait_lgkm0();
  issue(IC<K5_R>{});
  static_for<0, 14>([&](auto S) {
    constexpr int s = decltype(S)::v;
    if constexpr (s + 1 < 14) {
      constexpr int last = s + K5_R < 13 ? s + K5_R : 13;
      wait_vm<9 * (last - (s + 1)) + (s + 1 < K5_R ? 5 : 0)>();
      rd(IC<s + 1>{});
    }
    __builtin_amdgcn_sched_barrier(0);
#pragma unroll
    for (int j = 0; j < 5; ++j) acc[0][j] = mb16(wb[s & 1][j], gb[s & 1][0], acc[0][j]);
    __builtin_amdgcn_sched_barrier(0);
    if constexpr (s + 1 + K5_R < 14) { wait_lgkm0(); issue(IC<s + 1 + K5_R>{}); }
    __builtin_amdgcn_sched_barrier(0);
#pragma unroll
    for (int i = 1; i < 4; ++i)
#pragma unroll
      for (int j = 0; j < 5; ++j) acc[i][j] = mb16(wb[s & 1][j], gb[s & 1][i], acc[i][j]);
    __builtin_amdgcn_sched_barrier(0);
  });
  STAMP(ws, 5, 3);
  f32x4* red = (f32x4*)(smem + w * K5_RING) + lane;
#pragma unroll
  for (int i = 1; i < 4; ++i)
#pragma unroll
    for (int j = 0; j < 5; ++j) red[((i - 1) * 5 + j) * 64] = acc[i][j];
  STAMP(ws, 5, 4);
  asm volatile("s_waitcnt vmcnt(0) lgkmcnt(0)" : "+v"(hv[0]), "+v"(hv[1]), "+v"(hv[2]), "+v"(hv[3]), "+v"(hv[4])::"memory");
  raw_bar();
  STAMP(ws, 5, 5);
  u16* op = out + (size_t)mrow * 640 + ncol;
#pragma unroll
  for (int j = 0; j < 5; ++j) {
    f32x4 own = acc[0][j];
#pragma unroll
    for (int i = 1; i < 4; ++i)
      own += *((const f32x4*)(smem + ((w + 4 - i) & 3) * K5_RING) + ((i - 1) * 5 + j) * 64 + lane);
    const u32 o0 = pk_bf16(own[0], own[1]), o1 = pk_bf16(own[2], own[3]);
    u32x2 o = {pk_bf16(bf_lo(hv[j][0]) + bf_lo(o0), bf_hi(hv[j][0]) + bf_hi(o0)),
               pk_bf16(bf_lo(hv[j][1]) + bf_lo(o1), bf_hi(hv[j][1]) + bf_hi(o1))};
    *(u32x2*)(op + 16 * j) = o;
  }
  STAMP(ws, 5, 13);
  STAMPE(ws, 5);
}

extern "C" void kernel_launch(void* const* d_in, const int* in_sizes, int n_in, void* d_out, int out_size, void* d_ws,
                              size_t ws_size, hipStream_t stream) {
  const u16* x = (const u16*)d_in[0];
  const u16* wr = (const u16*)d_in[1];
  const u16* wi = (const u16*)d_in[2];
  const u16* wg = (const u16*)d_in[4];
  const u16* wu = (const u16*)d_in[5];
  const u16* wd = (const u16*)d_in[6];
  char* ws = (char*)d_ws;
  k1_kernel<<<K1_GRID, 256, 0, stream>>>(x, wr, wi, ws);
  k2_kernel<<<K2_GRID, 256, 0, stream>>>(wg, wu, ws);
  k3_kernel<<<K3_GRID, 256, 0, stream>>>(x, wd, ws);
  k4_kernel<<<256, 256, 0, stream>>>(ws);
  k5_kernel<<<256, 256, 0, stream>>>(ws, (u16*)d_out);
}
```

```cpp
#include <hip/hip_runtime.h>
#include <hip/hip_bf16.h>
#include <stdint.h>

#define DI __device__ __forceinline__
typedef unsigned short u16;
typedef uint32_t u32;
typedef _Float16 f16;
typedef f16 f16x2 __attribute__((ext_vector_type(2)));
typedef f16 f16x4 __attribute__((ext_vector_type(4)));
typedef f16 f16x8 __attribute__((ext_vector_type(8)));
typedef __bf16 bf16x2 __attribute__((ext_vector_type(2)));
typedef __bf16 bf16x8 __attribute__((ext_vector_type(8)));
typedef short s16x4 __attribute__((ext_vector_type(4)));
typedef float f32x2 __attribute__((ext_vector_type(2)));
typedef float f32x4 __attribute__((ext_vector_type(4)));
typedef float f32x16 __attribute__((ext_vector_type(16)));
typedef u32 u32x2 __attribute__((ext_vector_type(2)));
typedef u32 u32x4 __attribute__((ext_vector_type(4)));
typedef int i32x4 __attribute__((ext_vector_type(4)));
#define LAS __attribute__((address_space(3)))

constexpr int NCOLWG = 162, NSLOT = 4 * NCOLWG;
constexpr size_t X1_OFF = 0;
constexpr size_t X1_SZ = 256ull * NSLOT * 16;
constexpr size_t WT_OFF = X1_OFF + X1_SZ;
constexpr size_t WT_SZ = 323ull * 8192;
constexpr size_t X2_OFF = WT_OFF + WT_SZ;
constexpr size_t X2_SZ = 160ull * 16384;
constexpr size_t EC_OFF = X2_OFF + X2_SZ;
constexpr size_t EC_SZ = 4096;
constexpr size_t H_OFF = EC_OFF + EC_SZ;
constexpr size_t H_SZ = 2048ull * 640 * 2;
constexpr size_t ACT_OFF = H_OFF + H_SZ;
constexpr size_t ACT_SZ = 64ull * 40 * 1024;
constexpr size_t W1_OFF = ACT_OFF + ACT_SZ;
constexpr size_t W1_SZ = 16ull * 280 * 1024;
constexpr size_t W2_OFF = W1_OFF + W1_SZ;
constexpr size_t W2_SZ = 8ull * 56 * 5 * 1024;
constexpr size_t G_OFF = W2_OFF + W2_SZ;
constexpr size_t G_SZ = 32ull * 56 * 4 * 1024;
constexpr size_t CNT_OFF = G_OFF + G_SZ;

#define STAMPN(ws_, k_, i_) do {} while (0)
#define STAMPF(ws_, k_, i_) do {} while (0)
#define STAMP(ws_, k_, i_) do {} while (0)
#define STAMPE(ws_, k_) do {} while (0)
DI float asf(u32 u) { return __builtin_bit_cast(float, u); }
DI u32 asu(float f) { return __builtin_bit_cast(u32, f); }
DI u32 pk_bf16(float a, float b) { f32x2 v = {a, b}; return __builtin_bit_cast(u32, __builtin_convertvector(v, bf16x2)); }
DI u32 pk_f16(float a, float b) { f32x2 v = {a, b}; return __builtin_bit_cast(u32, __builtin_convertvector(v, f16x2)); }
DI u32 pk_f16v(f32x2 v) { return __builtin_bit_cast(u32, __builtin_convertvector(v, f16x2)); }
DI float bf_lo(u32 u) { return asf(u << 16); }
DI float bf_hi(u32 u) { return asf(u & 0xffff0000u); }
DI float bfr(float a) { return bf_lo(pk_bf16(a, a)); }
DI float cosr(float rev) { return __builtin_amdgcn_cosf(rev); }
DI float sinr(float rev) { return __builtin_amdgcn_sinf(rev); }
constexpr __host__ __device__ int crow(int reg, int h) { return (reg & 3) + 8 * (reg >> 2) + 4 * h; }
DI u32 umax(u32 a, u32 b) { return a > b ? a : b; }

template <int CTRL> DI u32 dppu(u32 v) { return (u32)__builtin_amdgcn_update_dpp(0, (int)v, CTRL, 0xf, 0xf, true); }
template <int CTRL> DI float dpp(float v) { return asf(dppu<CTRL>(asu(v))); }
DI float sum32(float v) {
  v += dpp<0xB1>(v); v += dpp<0x4E>(v); v += dpp<0x141>(v); v += dpp<0x140>(v);
  auto r = __builtin_amdgcn_permlane16_swap(asu(v), asu(v), false, false);
  return asf(r[0]) + asf(r[1]);
}
DI float sum64(float v) {
  v = sum32(v);
  auto r = __builtin_amdgcn_permlane32_swap(asu(v), asu(v), false, false);
  return asf(r[0]) + asf(r[1]);
}
DI u32 max64(u32 v) {
  v = umax(v, dppu<0xB1>(v)); v = umax(v, dppu<0x4E>(v)); v = umax(v, dppu<0x141>(v)); v = umax(v, dppu<0x140>(v));
  auto r = __builtin_amdgcn_permlane16_swap(v, v, false, false);
  v = umax(r[0], r[1]);
  auto s = __builtin_amdgcn_permlane32_swap(v, v, false, false);
  return umax(s[0], s[1]);
}
DI f16x8 asf16x8(u32x4 v) { return __builtin_bit_cast(f16x8, v); }
DI f32x16 mf16(u32x4 a, u32x4 b, f32x16 c) { return __builtin_amdgcn_mfma_f32_32x32x16_f16(asf16x8(a), asf16x8(b), c, 0, 0, 0); }
DI f32x16 mb32(u32x4 a, u32x4 b, f32x16 c) {
  return __builtin_amdgcn_mfma_f32_32x32x16_bf16(__builtin_bit_cast(bf16x8, a), __builtin_bit_cast(bf16x8, b), c, 0, 0, 0);
}
DI f32x4 mb16(u32x4 a, u32x4 b, f32x4 c) {
  return __builtin_amdgcn_mfma_f32_16x16x32_bf16(__builtin_bit_cast(bf16x8, a), __builtin_bit_cast(bf16x8, b), c, 0, 0, 0);
}
DI u32x2 tr_read(const char* p) {
  s16x4 r = __builtin_amdgcn_ds_read_tr16_b64_v4i16((LAS s16x4*)(p));
  return __builtin_bit_cast(u32x2, r);
}
DI f32x16 zero16() { f32x16 z; for (int i = 0; i < 16; ++i) z[i] = 0.f; return z; }
DI int wave_id() { return __builtin_amdgcn_readfirstlane(threadIdx.x >> 6); }
DI f32x2 lo2(const f32x16& v, int i) { return f32x2{v[i], v[i + 1]}; }
DI f32x2 pr2(const f32x4& v, int i) { return f32x2{v[i], v[i + 1]}; }
DI u32 pkmul_h(u32 a, u32 b) {
  const f16x2 r = __builtin_bit_cast(f16x2, a) * __builtin_bit_cast(f16x2, b);
  return __builtin_bit_cast(u32, r);
}
DI u32 pow2_h2(int d) {
  const float s = __builtin_amdgcn_ldexpf(1.f, d);
  return pk_f16(s, s);
}
template <class T> DI void pin(T& v) { asm volatile("" : "+v"(v)); }

DI void ln_scalars(float mean, float var, float& mb, float& inv) {
  mb = bfr(mean);
  float vb = bfr(var);
  float t = bfr(vb + 1e-5f);
  float den = bfr(__builtin_amdgcn_sqrtf(t));
  inv = __builtin_amdgcn_rcpf(den);
}

template <int N> struct IC { static constexpr int v = N; };
template <int I, int N, class F> DI void static_for(F&& f) {
  if constexpr (I < N) { f(IC<I>{}); static_for<I + 1, N>(f); }
}
template <int N> DI void wait_vm() { asm volatile("s_waitcnt vmcnt(%0)" ::"n"(N) : "memory"); }
DI void wait_lgkm0() { asm volatile("s_waitcnt lgkmcnt(0)" ::: "memory"); }
DI void raw_bar() { asm volatile("s_barrier" ::: "memory"); }
DI u32 lds_addr(const char* p) { return (u32)(size_t)(LAS const char*)p; }

template <int N> DI void dma(u32 voff, const void* g, u32 lds) {
  if constexpr (N == 4)
    asm volatile("s_mov_b32 m0, %2\n\ts_nop 0\n\tglobal_load_lds_dwordx4 %0, %1\n\t"
                 "global_load_lds_dwordx4 %0, %1 offset:1024\n\tglobal_load_lds_dwordx4 %0, %1 offset:2048\n\t"
                 "global_load_lds_dwordx4 %0, %1 offset:3072" ::"v"(voff), "s"(g), "s"(lds) : "memory");
  else if constexpr (N == 3)
    asm volatile("s_mov_b32 m0, %2\n\ts_nop 0\n\tglobal_load_lds_dwordx4 %0, %1\n\t"
                 "global_load_lds_dwordx4 %0, %1 offset:1024\n\tglobal_load_lds_dwordx4 %0, %1 offset:2048" ::"v"(voff),
                 "s"(g), "s"(lds) : "memory");
  else if constexpr (N == 2)
    asm volatile("s_mov_b32 m0, %2\n\ts_nop 0\n\tglobal_load_lds_dwordx4 %0, %1\n\t"
                 "global_load_lds_dwordx4 %0, %1 offset:1024" ::"v"(voff), "s"(g), "s"(lds) : "memory");
  else
    asm volatile("s_mov_b32 m0, %2\n\ts_nop 0\n\tglobal_load_lds_dwordx4 %0, %1" ::"v"(voff), "s"(g), "s"(lds) : "memory");
}
DI void st16_dev(u32 voff, const void* g, u32x4 v) {
  asm volatile("global_store_dwordx4 %0, %1, %2 sc1\n\ts_nop 1" ::"v"(voff), "v"(v), "s"(g) : "memory");
}
DI void st8_dev(u32 voff, const void* g, u32x2 v) {
  asm volatile("global_store_dwordx2 %0, %1, %2 sc1" ::"v"(voff), "v"(v), "s"(g) : "memory");
}
DI void ld4_dev(u32x4& a0, u32x4& a1, u32x4& a2, u32x4& a3, u32 voff, const void* g) {
  asm volatile("global_load_dwordx4 %0, %4, %5 sc1\n\tglobal_load_dwordx4 %1, %4, %5 offset:1024 sc1\n\t"
               "global_load_dwordx4 %2, %4, %5 offset:2048 sc1\n\tglobal_load_dwordx4 %3, %4, %5 offset:3072 sc1"
               : "=&a"(a0), "=&a"(a1), "=&a"(a2), "=&a"(a3) : "v"(voff), "s"(g) : "memory");
}
DI void ld1_dev(u32x4& a0, u32 voff, const void* g) {
  asm volatile("global_load_dwordx4 %0, %1, %2 sc1" : "=&a"(a0) : "v"(voff), "s"(g) : "memory");
}
DI void ldh_dev(u32x2 (&hv)[5], u32 vo, const void* g) {
  asm volatile("global_load_dwordx2 %0, %5, %6 sc1\n\tglobal_load_dwordx2 %1, %5, %6 offset:32 sc1\n\t"
               "global_load_dwordx2 %2, %5, %6 offset:64 sc1\n\tglobal_load_dwordx2 %3, %5, %6 offset:96 sc1\n\t"
               "global_load_dwordx2 %4, %5, %6 offset:128 sc1"
               : "=&v"(hv[0]), "=&v"(hv[1]), "=&v"(hv[2]), "=&v"(hv[3]), "=&v"(hv[4]) : "v"(vo), "s"(g) : "memory");
}
template <int N> DI void wait_a2(u32x4& a0, u32x4& a1) {
  asm volatile("s_waitcnt vmcnt(%2)" : "+a"(a0), "+a"(a1) : "n"(N) : "memory");
}
DI float vmul(float a, float b) { float d; asm volatile("v_mul_f32 %0, %1, %2" : "=v"(d) : "v"(a), "v"(b)); return d; }
DI float vpoly(float z, float k1) { float d; asm volatile("v_fmamk_f32 %0, %1, 0xbdd2d3e8, %2" : "=v"(d) : "v"(z), "v"(k1)); return d; }
DI float vadd1(float a) { float d; asm volatile("v_add_f32 %0, 1.0, %1" : "=v"(d) : "v"(a)); return d; }
DI float vexp(float a) { float d; asm volatile("v_exp_f32 %0, %1" : "=v"(d) : "v"(a)); return d; }
DI float vrcp(float a) { float d; asm volatile("v_rcp_f32 %0, %1" : "=v"(d) : "v"(a)); return d; }
template <bool FIRST> DI void mfma32(f32x16& d, const u32x4& a, const u32x4& b) {
  if constexpr (FIRST) asm volatile("v_mfma_f32_32x32x16_bf16 %0, %1, %2, 0" : "=&v"(d) : "v"(a), "a"(b));
  else asm volatile("v_mfma_f32_32x32x16_bf16 %0, %1, %2, %0" : "+v"(d) : "v"(a), "a"(b));
}
template <bool FIRST> DI void mfma16(f32x4& d, const u32x4& a, const u32x4& b) {
  if constexpr (FIRST) asm volatile("v_mfma_f32_16x16x32_bf16 %0, %1, %2, 0" : "=&v"(d) : "v"(a), "a"(b));
  else asm volatile("v_mfma_f32_16x16x32_bf16 %0, %1, %2, %0" : "+v"(d) : "v"(a), "a"(b));
}
DI void signal(u32* cnt, int tid) {
  if (tid == 0) __hip_atomic_fetch_add(cnt, 1u, __ATOMIC_RELAXED, __HIP_MEMORY_SCOPE_AGENT);
}
DI void poll_issue(u32& v, const u32* cnt) {
  asm volatile("global_load_dword %0, %1, %2 sc1" : "=&v"(v) : "v"(0), "s"(cnt) : "memory");
}
template <int N> DI void poll_check(u32& v, u32* cnt) {
  asm volatile("s_waitcnt vmcnt(%1)" : "+v"(v) : "n"(N) : "memory");
  if (__builtin_amdgcn_readfirstlane(v) < 16u) {
    for (int it = 0; it < (1 << 20); ++it) {
      if (__builtin_amdgcn_readfirstlane(__hip_atomic_load(cnt, __ATOMIC_RELAXED, __HIP_MEMORY_SCOPE_AGENT)) >= 16u) break;
      __builtin_amdgcn_s_sleep(1);
    }
  }
  asm volatile("" ::: "memory");
}
DI void tstamp(unsigned long long& t) { asm volatile("s_memtime %0\n\ts_waitcnt lgkmcnt(0)" : "=s"(t)::"memory"); }
#define PW_STAMP
#define PW_STAMP2
template <class F> DI void publish_and_wait(u32* cnt, int tid, int w, F&& idle) {
  asm volatile("s_waitcnt vmcnt(0) lgkmcnt(0)" ::: "memory");
  raw_bar();
  PW_STAMP2;
  if (w == 0) {
    if (tid == 0) __hip_atomic_fetch_add(cnt, 1u, __ATOMIC_RELAXED, __HIP_MEMORY_SCOPE_AGENT);
    for (int it = 0; it < (1 << 20); ++it) {
      const u32 v = __builtin_amdgcn_readfirstlane(__hip_atomic_load(cnt, __ATOMIC_RELAXED, __HIP_MEMORY_SCOPE_AGENT));
      if (v >= 16u) break;
      __builtin_amdgcn_s_sleep(1);
    }
    PW_STAMP;
  } else idle();
  raw_bar();
}

constexpr double PI_ = 3.14159265358979323846;
constexpr double ccos_(double x) {
  double t = 1, s = 1;
  for (int k = 1; k <= 16; ++k) { t *= -x * x / ((2 * k - 1) * (2 * k)); s += t; }
  return s;
}
constexpr double c2048(int k) { k &= 2047; k = k > 1024 ? 2048 - k : k; return ccos_(2 * PI_ * k / 2048); }
constexpr u32 fb(double x) { return __builtin_bit_cast(u32, (float)x); }

struct ColTask { int a1, b1, am, bm; bool valid; };
constexpr __host__ __device__ ColTask col_task(int g, int p) {
  if (g < 150) { const int a1 = g / 10 + 1, b1 = 2 * (g % 10) + p; return {a1, b1, 32 - a1, 19 - b1, true}; }
  if (g < 155) { const int b1 = 2 * (g - 150) + p; return {16, b1, 16, 19 - b1, true}; }
  if (g == 155) return {0, 0, 0, 0, p == 0};
  if (g == 156) return {0, 10, 0, 10, p == 0};
  const int b1 = 2 * (g - 157) + 1 + p;
  return {0, b1 < 10 ? b1 : 9, 0, b1 < 10 ? 20 - b1 : 11, b1 < 10};
}
struct SlotMap {
  u32 v[768];
  constexpr SlotMap() : v() {
    for (int slot = 0; slot < NSLOT; ++slot) {
      const int mir = (slot >> 1) & 1;
      const ColTask t = col_task(slot >> 2, slot & 1);
      const int q1 = t.a1 + 32 * t.b1, a = mir ? t.am : t.a1, b = mir ? t.bm : t.b1;
      const bool cj = mir ? !(q1 > 320) : (q1 > 320);
      v[slot] = (u32)(33 * b + a) | (cj ? 0x80000000u : 0u);
    }
  }
};
template <int SD> struct ThetaTab {
  u32 v[4 * 256];
  constexpr ThetaTab() : v() {
    for (int l = 0; l < 64; ++l)
      for (int n = 0; n < 8; ++n) {
        const int p = 16 * SD + (l & 15) + 32 * crow(4 * (n >> 1) + 2 * ((l >> 4) & 1) + (n & 1), l >> 5);
        v[((n >> 2) * 64 + l) * 4 + (n & 3)] = fb(c2048(p));
        v[((2 + (n >> 2)) * 64 + l) * 4 + (n & 3)] = fb(c2048(p + 1536));
      }
  }
};
struct Theta { ThetaTab<0> a; ThetaTab<1> b; };
static __device__ const __attribute__((aligned(128))) SlotMap g_smap{};
static __device__ const __attribute__((aligned(128))) Theta g_theta{};

DI void dft32_consts(float (&c)[16], float (&s)[16], int r, int h) {
  const float rf = (float)r * (1.f / 32.f), r4 = rf * (float)(4 * h);
#pragma unroll
  for (int i = 0; i < 16; ++i) { const float rev = rf * (float)crow(i, 0) + r4; c[i] = cosr(rev); s[i] = sinr(rev); }
}
DI void pack_csn(const float (&c)[16], const float (&s)[16], u32x4 (&Cp)[2], u32x4 (&Sp)[2], u32x4 (&Np)[2]) {
#pragma unroll
  for (int i = 0; i < 8; ++i) {
    Cp[i >> 2][i & 3] = pk_f16(c[2 * i], c[2 * i + 1]);
    Sp[i >> 2][i & 3] = pk_f16(s[2 * i], s[2 * i + 1]);
    Np[i >> 2][i & 3] = pk_f16(-s[2 * i], -s[2 * i + 1]);
  }
}

constexpr int K1_IMG = 1344;
constexpr int K1_PLANE = 33 * 20 * 4;
constexpr int K1_PL0 = 8 * K1_IMG;
constexpr int K1_LDS = 22528;
constexpr int K1_WT_WGS = 128;
constexpr int K1_W2_PITCH = 672;
constexpr int K1_GRID = 256 + K1_WT_WGS + 112;
static_assert(32 * K1_W2_PITCH <= 22528, "LDS");
static_assert(K1_PL0 + 4 * K1_PLANE <= K1_LDS, "LDS");

DI void k1_row(const u16* __restrict__ x, char* __restrict__ X1, char* smem, int wg) {
  const int tid = threadIdx.x, lane = tid & 63, w = wave_id(), r = lane & 31, h = lane >> 5;
  const int b2 = wg >> 3, aq = wg & 7;
  const int m = 32 * (4 * aq + w) + b2;
  u32x2 v[5];
  u32 e[3];
  {
    const char* src = (const char*)x + (size_t)m * 2560;
    const u32 vo = h * 1280 + r * 8;
    asm volatile("global_load_dwordx2 %0, %5, %6\n\tglobal_load_dwordx2 %1, %5, %6 offset:256\n\t"
                 "global_load_dwordx2 %2, %5, %6 offset:512\n\tglobal_load_dwordx2 %3, %5, %6 offset:768\n\t"
                 "global_load_dwordx2 %4, %5, %6 offset:1024"
                 : "=&v"(v[0]), "=&v"(v[1]), "=&v"(v[2]), "=&v"(v[3]), "=&v"(v[4]) : "v"(vo), "s"(src) : "memory");
    asm volatile("global_load_dword %0, %3, %4\n\tglobal_load_dword %1, %3, %4 offset:1024\n\t"
                 "global_load_dword %2, %3, %4 offset:2048"
                 : "=&v"(e[0]), "=&v"(e[1]), "=&v"(e[2]) : "v"(tid * 4), "s"(g_smap.v) : "memory");
  }
  u32x4 C1[2], S1[2], N1[2], C2, S2, N2, MR, MI;
  f32x2 tc[6], ts[6];
  {
    float c[16], s[16];
    dft32_consts(c, s, r, h);
    pack_csn(c, s, C1, S1, N1);
    const float rf = (float)r * (1.f / 640.f), r4 = rf * (float)(4 * h);
#pragma unroll
    for (int i = 0; i < 12; ++i) {
      const float rev = rf * (float)crow(i, 0) + r4;
      tc[i >> 1][i & 1] = cosr(rev); ts[i >> 1][i & 1] = sinr(rev);
      const float rev2 = rev * 32.f;
      c[i] = cosr(rev2); s[i] = sinr(rev2);
    }
#pragma unroll
    for (int i = 0; i < 4; ++i) {
      C2[i] = pk_f16(c[2 * i], c[2 * i + 1]); S2[i] = pk_f16(s[2 * i], s[2 * i + 1]); N2[i] = pk_f16(-s[2 * i], -s[2 * i + 1]);
    }
#pragma unroll
    for (int i = 0; i < 2; ++i) {
      const u32 cc = h ? 0u : pk_f16(c[8 + 2 * i], c[9 + 2 * i]), ss = h ? 0u : pk_f16(s[8 + 2 * i], s[9 + 2 * i]);
      MR[i] = cc; MR[2 + i] = ss; MI[i] = ss ^ 0x80008000u; MI[2 + i] = cc;
    }
#pragma unroll
    for (int i = 0; i < 2; ++i) { pin(C1[i]); pin(S1[i]); pin(N1[i]); }
    pin(C2); pin(S2); pin(N2); pin(MR); pin(MI);
#pragma unroll
    for (int i = 0; i < 6; ++i) { pin(tc[i]); pin(ts[i]); }
  }
  STAMP(X1 - X1_OFF, 1, 5);
  asm volatile("s_waitcnt vmcnt(3)" : "+v"(v[0]), "+v"(v[1]), "+v"(v[2]), "+v"(v[3]), "+v"(v[4])::"memory");
  STAMP(X1 - X1_OFF, 1, 1);
  f32x2 f[10];
#pragma unroll
  for (int i = 0; i < 5; ++i) {
    f[2 * i] = f32x2{bf_lo(v[i][0]), bf_hi(v[i][0])};
    f[2 * i + 1] = f32x2{bf_lo(v[i][1]), bf_hi(v[i][1])};
  }
  f32x2 s2 = f[0];
#pragma unroll
  for (int i = 1; i < 10; ++i) s2 += f[i];
  const float mean = sum32(s2[0] + s2[1]) * (1.f / 640.f);
  f32x2 q2 = {0.f, 0.f};
#pragma unroll
  for (int i = 0; i < 10; ++i) { const f32x2 d = f[i] - mean; q2 += d * d; }
  const float var = sum32(q2[0] + q2[1]) * (1.f / 640.f);
  float mb, inv;
  ln_scalars(mean, var, mb, inv);
  char* img = smem + w * (2 * K1_IMG);
#pragma unroll
  for (int i = 0; i < 5; ++i) {
    u32x2 o;
#pragma unroll
    for (int k = 0; k < 2; ++k) {
      const f32x2 d0 = f[2 * i + k] - mb;
      const u32 d = pk_bf16(d0[0], d0[1]);
      const f32x2 n0 = f32x2{bf_lo(d), bf_hi(d)} * inv;
      const u32 n = pk_bf16(n0[0], n0[1]);
      o[k] = pk_f16(bf_lo(n), bf_hi(n));
    }
    *(u32x2*)(img + h * K1_IMG + 8 * (r + 32 * i)) = o;
  }
  if (r < 8) { u32x2 z = {0u, 0u}; *(u32x2*)(img + h * K1_IMG + 1280 + 8 * r) = z; }
  asm volatile("" ::: "memory");
  STAMP(X1 - X1_OFF, 1, 2);
  const int q4 = (lane & 15) >> 2, p4 = lane & 3, blk = (lane >> 4) & 1;
  const char* tb = img + (4 * h + q4) * 40 + 32 * blk + 8 * p4;
  f32x16 Dr = zero16(), Di = zero16();
#pragma unroll
  for (int s = 0; s < 2; ++s) {
    u32x2 r0 = tr_read(tb + 640 * s), r1 = tr_read(tb + 640 * s + 320);
    u32x2 i0 = tr_read(tb + K1_IMG + 640 * s), i1 = tr_read(tb + K1_IMG + 640 * s + 320);
    u32x4 ar = {r0[0], r0[1], r1[0], r1[1]}, ai = {i0[0], i0[1], i1[0], i1[1]};
    Dr = mf16(ar, C1[s], Dr); Di = mf16(ai, C1[s], Di);
    Dr = mf16(ai, S1[s], Dr); Di = mf16(ar, N1[s], Di);
  }
  u32 er[6], ei[6];
#pragma unroll
  for (int k = 0; k < 6; ++k) {
    const f32x2 dr = lo2(Dr, 2 * k), di = lo2(Di, 2 * k);
    er[k] = pk_f16v(dr * tc[k] + di * ts[k]);
    ei[k] = pk_f16v(di * tc[k] - dr * ts[k]);
  }
  const u32x4 pr = {er[0], er[1], er[2], er[3]}, pi = {ei[0], ei[1], ei[2], ei[3]}, pm = {er[4], er[5], ei[4], ei[5]};
  f32x16 Xr = zero16(), Xi = zero16();
  Xr = mf16(C2, pr, Xr); Xi = mf16(N2, pr, Xi);
  Xr = mf16(S2, pi, Xr); Xi = mf16(C2, pi, Xi);
  Xr = mf16(MR, pm, Xr); Xi = mf16(MI, pm, Xi);
  char* planes = smem + K1_PL0;
  u32* pl = (u32*)(planes + w * K1_PLANE) + 33 * 4 * h + r;
#pragma unroll
  for (int i = 0; i < 8; ++i) pl[33 * crow(i, 0)] = pk_f16(Xr[i], Xi[i]);
  if (h == 0) {
#pragma unroll
    for (int i = 8; i < 12; ++i) pl[33 * crow(i, 0)] = pk_f16(Xr[i], Xi[i]);
  }
  STAMP(X1 - X1_OFF, 1, 3);
  asm volatile("s_waitcnt vmcnt(0) lgkmcnt(0)" : "+v"(e[0]), "+v"(e[1]), "+v"(e[2])::"memory");
  raw_bar();
  STAMP(X1 - X1_OFF, 1, 4);
  u32x4* dst = (u32x4*)(X1 + (size_t)(wg >> 1) * (NSLOT * 32)) + (aq & 1) * 4 + (tid >> 2) * 8 + (tid & 3);
#pragma unroll
  for (int it = 0; it < 3; ++it) {
    if (it < 2 || tid < NSLOT - 512) {
      const u32* sp = (const u32*)planes + (e[it] & 0xffffu);
      const u32 cj = e[it] & 0x80000000u;
      u32x4 o = {sp[0] ^ cj, sp[K1_PLANE / 4] ^ cj, sp[2 * (K1_PLANE / 4)] ^ cj, sp[3 * (K1_PLANE / 4)] ^ cj};
      dst[512 * it] = o;
    }
  }
}

DI int wt_off(int i, int h, int a) { return (((a >> 4) * 4 + (i >> 1)) * 64 + h * 32 + (i & 1) * 16 + (a & 15)) * 16; }

DI void k1_wt(const u16* __restrict__ wr, const u16* __restrict__ wi, char* __restrict__ WT, char* smem, int wg) {
  const int tid = threadIdx.x, lane = tid & 63, w = wave_id();
  const int qh = wg & 1, ao = (wg >> 1) & 3, h = (wg >> 3) & 1, i = wg >> 4;
  const int p0 = 8 * ao + 32 * (crow(2 * i, 0) + 4 * h), q0 = 161 * qh, nq = 161 - qh;
  {
    const char* src = (const char*)(w >> 1 ? wi : wr);
    const u32 lds = lds_addr(smem) + (w >> 1) * 11264;
#pragma unroll
    for (int n = 0; n < 6; ++n) {
      const int ins = (w & 1) * 6 + n;
      if (ins < 11) {
        const int e = ins * 64 + lane, row = e / 22, c = e - 22 * row;
        const int p = p0 + (row & 7) + ((row >> 3) & 1) * 1024 + (row >> 4) * 32;
        const u32 vo = min((u32)((642 * p + 2 * q0) & ~15) + 16u * c, 2048u * 642u - 16u);
        dma<1>(vo, src, lds + ins * 1024);
      }
    }
  }
  wait_vm<0>();
  raw_bar();
  const int alo = tid & 7, a = 8 * ao + alo;
  const char* lb = smem + alo * 352 + 2 * ((alo + q0) & 7) + 2 * (tid >> 3);
  char* ob = WT + (size_t)(q0 + (tid >> 3)) * 8192 + wt_off(i, h, a);
#pragma unroll
  for (int it = 0; it < 6; ++it) {
    const int ql = (tid >> 3) + 32 * it;
    if (ql < nq) {
      u32x4 o;
#pragma unroll
      for (int d = 0; d < 4; ++d)
        o[d] = (u32) * (const u16*)(lb + 64 * it + d * 2816) | ((u32) * (const u16*)(lb + 64 * it + d * 2816 + 11264) << 16);
      *(u32x4*)(ob + (size_t)it * (32 * 8192)) = o;
      if (it == 0 || it == 4) {
        const int q = q0 + ql;
        if (q == 0 || q == 320) {
          char* mb = WT + (size_t)(q ? 322 : 321) * 8192;
#pragma unroll
          for (int d = 0; d < 4; ++d) {
            const int k = p0 + alo + (d & 1) * 1024 + (d >> 1) * 32, kp = (2048 - k) & 2047;
            const int pp = kp & 1023, half = kp >> 10, a2 = pp & 31, bb = pp >> 5;
            const int e = (bb & 3) + 4 * (bb >> 3);
            *(u32*)(mb + wt_off(e >> 1, (bb >> 2) & 1, a2) + (e & 1) * 8 + half * 4) = o[d];
          }
        }
      }
    }
  }
}

DI void k1_w2(const u16* __restrict__ wd, char* __restrict__ W2, char* smem, int idx) {
  const int tid = threadIdx.x, lane = tid & 63, w = wave_id();
  const int k32 = idx >> 1, nh = idx & 1;
  u32x4 v[5];
#pragma unroll
  for (int it = 0; it < 5; ++it) {
    const int e = tid + 256 * it;
    const int row = e / 40, c = e - 40 * row;
    const int e2 = row >> 4;
    const int t = k32 < 32 ? k32 >> 1 : (k32 < 48 ? k32 - 32 : 2 * (k32 - 48) + e2);
    const int ft = k32 < 32 ? 2 * (k32 & 1) + e2 : (k32 < 48 ? 4 + e2 : 6), fl = 16 * ft + (row & 15);
    u32x4 z = {0u, 0u, 0u, 0u};
    v[it] = fl < 108 ? *(const u32x4*)(wd + (size_t)(108 * t + fl) * 640 + 320 * nh + 8 * c) : z;
  }
#pragma unroll
  for (int it = 0; it < 5; ++it) {
    const int e = tid + 256 * it;
    const int row = e / 40, c = e - 40 * row;
    *(u32x4*)(smem + row * K1_W2_PITCH + 16 * c) = v[it];
  }
  __syncthreads();
  const int g = lane >> 4, q4 = (lane & 15) >> 2, p4 = lane & 3;
  const char* lb = smem + (16 * (g >> 1) + 4 * (g & 1) + q4) * K1_W2_PITCH + 8 * p4;
#pragma unroll
  for (int i = 0; i < 5; ++i) {
    const int fr = w * 5 + i;
    u32x2 lo = tr_read(lb + 32 * fr), hi = tr_read(lb + 8 * K1_W2_PITCH + 32 * fr);
    u32x4 o = {lo[0], lo[1], hi[0], hi[1]};
    *((u32x4*)(W2 + ((size_t)((4 * nh + w) * 56 + k32) * 5 + i) * 1024) + lane) = o;
  }
}

__global__ __launch_bounds__(256, 2) void k1_kernel(const u16* __restrict__ x, const u16* __restrict__ wr,
                                                    const u16* __restrict__ wi, const u16* __restrict__ wd, char* __restrict__ ws) {
  __shared__ __attribute__((aligned(16))) char smem[K1_LDS];
  const int bid = blockIdx.x;
  STAMP(ws, 1, 0);
  if (bid < 256) k1_row(x, ws + X1_OFF, smem, (bid & 7) * 32 + (bid >> 3));
  else if (bid < 256 + K1_WT_WGS) k1_wt(wr, wi, ws + WT_OFF, smem, bid - 256);
  else {
    if (bid == 256 + K1_WT_WGS && threadIdx.x < 48) *(u32*)(ws + CNT_OFF + threadIdx.x * 256) = 0u;
    k1_w2(wd, ws + W2_OFF, smem, bid - 256 - K1_WT_WGS);
  }
  STAMP(ws, 1, 13);
  STAMPE(ws, 1);
}

constexpr int K2_W1_PITCH = 288, K2_W1_MAT = 80 * K2_W1_PITCH + 128;
constexpr int K2_X = 0, K2_ST = 0, K2_W = 16384, K2_TH = 32768, K2_E1 = 40960, K2_E2 = 49152, K2_EB = 57344;
constexpr int K2_LDS = K2_EB + 64;
constexpr int K2_GRID = 168 + 128;

DI void k2_col(const char* __restrict__ X1, const char* __restrict__ WT, char* __restrict__ X2, unsigned char* __restrict__ ecol,
               char* smem, int wg) {
  const int tid = threadIdx.x, lane = tid & 63, w = wave_id(), r = lane & 31, h = lane >> 5;
  const int pw = w & 1, sd = w >> 1, rho = (lane >> 4) & 1;
  const u32 lds0 = lds_addr(smem);
  const u32 l16 = lane * 16;
  {
    const char* xb = X1 + (size_t)wg * 128 + (size_t)w * (NSLOT * 32);
#pragma unroll
    for (int n = 0; n < 4; ++n) {
      const int rr = n * 8 + (lane >> 3);
      const u32 vo = (u32)rr * (u32)(4 * NSLOT * 32) + (u32)(((lane & 7) ^ ((rr >> 1) & 7)) * 16);
      dma<1>(vo, xb, lds0 + K2_X + (w * 4 + n) * 1024);
    }
  }
  const ColTask tk = col_task(wg, pw);
  const int a1 = tk.a1, b1 = tk.b1, am = tk.am, bm = tk.bm;
  const bool valid = tk.valid;
  const bool direct = wg >= 155, self = wg == 155 || wg == 156;
  const int q1 = a1 + 32 * b1, qm = am + 32 * bm;
  const bool swp = q1 > 320;
  const int task = (self && pw) ? (wg == 155 ? 321 : 322) : (swp ? qm : q1);
  dma<4>(l16, WT + (size_t)task * 8192 + sd * 4096, lds0 + K2_W + (pw * 2 + sd) * 4096);
  dma<2>(l16, (const char*)&g_theta + w * 2048, lds0 + K2_TH + w * 2048);
  u32x4 BR[4], BI[4], Cp[2], Sp[2], Np[2];
  f32x2 tc[8], ts[8];
  {
    float c[16], s[16];
    dft32_consts(c, s, r, h);
    pack_csn(c, s, Cp, Sp, Np);
#pragma unroll
    for (int i = 0; i < 16; ++i) { BR[i >> 2][i & 3] = pk_f16(c[i], s[i]); BI[i >> 2][i & 3] = pk_f16(-s[i], c[i]); }
    const float rf = (float)r * (1.f / 1024.f), r4 = rf * (float)(4 * h);
#pragma unroll
    for (int i = 0; i < 16; ++i) {
      const float rev = rf * (float)crow(i, 0) + r4;
      tc[i >> 1][i & 1] = cosr(rev); ts[i >> 1][i & 1] = sinr(rev);
    }
#pragma unroll
    for (int i = 0; i < 4; ++i) { pin(BR[i]); pin(BI[i]); }
#pragma unroll
    for (int i = 0; i < 2; ++i) { pin(Cp[i]); pin(Sp[i]); pin(Np[i]); }
#pragma unroll
    for (int i = 0; i < 8; ++i) { pin(tc[i]); pin(ts[i]); }
  }
  STAMPN(X2 - X2_OFF, 2, 9);
  wait_vm<6>();
  raw_bar();
  STAMPF(X2 - X2_OFF, 2, 9);
  STAMP(X2 - X2_OFF, 2, 1);
  const int slot = (((sd == 1) != swp) ? 2 : 0) + pw;
  f32x16 Zr = zero16(), Zi = zero16();
  {
    const char* xr = smem + K2_X + r * 128 + (((h * 4 + slot) ^ ((r >> 1) & 7)) * 16);
#pragma unroll
    for (int ks = 0; ks < 4; ++ks) {
      const u32x4 a = *(const u32x4*)(xr + ks * 4096);
      Zr = mf16(a, BR[ks], Zr); Zi = mf16(a, BI[ks], Zi);
    }
  }
  STAMP(X2 - X2_OFF, 2, 2);
  u32x4 P[4];
#pragma unroll
  for (int k = 0; k < 8; ++k) {
    const f32x2 dr = lo2(Zr, 2 * k), di = lo2(Zi, 2 * k);
    P[k >> 2][k & 3] = pk_f16v(dr * tc[k] + di * ts[k]);
    P[2 + (k >> 2)][k & 3] = pk_f16v(di * tc[k] - dr * ts[k]);
  }
  {
    u32x4* e1w = (u32x4*)(smem + K2_E1 + w * 2048) + (h * 16 + (lane & 15));
    const u32x4* e1r = (const u32x4*)(smem + K2_E1 + (w ^ 2) * 2048) + (h * 16 + (lane & 15));
    if (rho != sd) {
#pragma unroll
      for (int f = 0; f < 4; ++f) e1w[32 * f] = P[f];
    }
    wait_vm<0>();
    wait_lgkm0();
    raw_bar();
    if (rho != sd) {
#pragma unroll
      for (int f = 0; f < 4; ++f) P[f] = e1r[32 * f];
    }
  }
  STAMP(X2 - X2_OFF, 2, 3);
  Zr = zero16(); Zi = zero16();
#pragma unroll
  for (int s = 0; s < 2; ++s) {
    Zr = mf16(Cp[s], P[s], Zr); Zi = mf16(Cp[s], P[2 + s], Zi);
    Zr = mf16(Sp[s], P[2 + s], Zr); Zi = mf16(Np[s], P[s], Zi);
  }
  f32x2 alr[4], ali[4], ber[4], bei[4], dcr[4], dci[4];
  {
    const u32x4* wl = (const u32x4*)(smem + K2_W + (pw * 2 + sd) * 4096) + lane;
    const u32x4* wsl = (const u32x4*)(smem + K2_W + (2 + sd) * 4096) + lane;
    const f32x4* th = (const f32x4*)(smem + K2_TH + sd * 4096) + lane;
#pragma unroll
    for (int g = 0; g < 4; ++g) {
      const u32x4 t = wl[64 * g];
      f32x2 w0r = {bf_lo(t[0]), bf_lo(t[2])}, w0i = {bf_hi(t[0]), bf_hi(t[2])};
      f32x2 w1r = {bf_lo(t[1]), bf_lo(t[3])}, w1i = {bf_hi(t[1]), bf_hi(t[3])};
      if (self) {
        const u32x4 u = wsl[64 * g];
        w0r = (w0r + f32x2{bf_lo(u[0]), bf_lo(u[2])}) * 0.5f; w0i = (w0i - f32x2{bf_hi(u[0]), bf_hi(u[2])}) * 0.5f;
        w1r = (w1r + f32x2{bf_lo(u[1]), bf_lo(u[3])}) * 0.5f; w1i = (w1i - f32x2{bf_hi(u[1]), bf_hi(u[3])}) * 0.5f;
      }
      const f32x4 thc = th[64 * (g >> 1)], ths = th[64 * (2 + (g >> 1))];
      const f32x2 ct = pr2(thc, 2 * (g & 1)), st = pr2(ths, 2 * (g & 1));
      const f32x2 sr = w0r + w1r, si = w0i + w1i, dr = w0r - w1r, di = w0i - w1i;
      alr[g] = sr - dr * st; ali[g] = si - di * st; ber[g] = sr + dr * st; bei[g] = si + di * st;
      dcr[g] = dr * ct; dci[g] = di * ct;
    }
  }
  STAMP(X2 - X2_OFF, 2, 4);
  f32x2 nur[4], nui[4], nvr[4], nvi[4];
  float mx = 0.f;
#pragma unroll
  for (int g = 0; g < 4; ++g) {
    f32x2 ur, ui, vr, vi;
#pragma unroll
    for (int k = 0; k < 2; ++k) {
      auto sr_ = __builtin_amdgcn_permlane16_swap(asu(Zr[4 * g + k]), asu(Zr[4 * g + 2 + k]), false, false);
      auto si_ = __builtin_amdgcn_permlane16_swap(asu(Zi[4 * g + k]), asu(Zi[4 * g + 2 + k]), false, false);
      ur[k] = asf(sr_[0]); vr[k] = asf(sr_[1]); ui[k] = asf(si_[0]); vi[k] = asf(si_[1]);
    }
    nur[g] = alr[g] * ur - ali[g] * ui - dcr[g] * vi - dci[g] * vr;
    nui[g] = alr[g] * ui + ali[g] * ur + dcr[g] * vr - dci[g] * vi;
    nvr[g] = ber[g] * vr - bei[g] * vi + dcr[g] * ui + dci[g] * ur;
    nvi[g] = ber[g] * vi + bei[g] * vr - dcr[g] * ur + dci[g] * ui;
#pragma unroll
    for (int k = 0; k < 2; ++k)
      mx = fmaxf(fmaxf(mx, fmaxf(fabsf(nur[g][k]), fabsf(nui[g][k]))), fmaxf(fabsf(nvr[g][k]), fabsf(nvi[g][k])));
  }
  const int eb = (int)umax(max64(asu(mx)) >> 23, 4u);
  const float sc = asf((u32)(258 - eb) << 23);
#pragma unroll
  for (int g = 0; g < 4; ++g) {
    auto qr = __builtin_amdgcn_permlane16_swap(pk_f16v(nur[g] * sc), pk_f16v(nvr[g] * sc), false, false);
    auto qi = __builtin_amdgcn_permlane16_swap(pk_f16v(nui[g] * sc), pk_f16v(nvi[g] * sc), false, false);
    P[g >> 1][2 * (g & 1)] = qr[0]; P[g >> 1][2 * (g & 1) + 1] = qr[1];
    P[2 + (g >> 1)][2 * (g & 1)] = qi[0]; P[2 + (g >> 1)][2 * (g & 1) + 1] = qi[1];
  }
  STAMP(X2 - X2_OFF, 2, 5);
  Zr = zero16(); Zi = zero16();
#pragma unroll
  for (int s = 0; s < 2; ++s) {
    Zr = mf16(P[s], Cp[s], Zr); Zi = mf16(P[s], Sp[s], Zi);
    Zr = mf16(P[2 + s], Np[s], Zr); Zi = mf16(P[2 + s], Cp[s], Zi);
  }
  int em;
  auto finish = [&](auto SD) {
    constexpr int S = decltype(SD)::v, KO = 8 * S, SO = 8 - KO;
    u32x4 kr, ki, orr, oi;
    u32x4* e2w = (u32x4*)(smem + K2_E2 + w * 2048) + lane;
    const u32x4* e2r = (const u32x4*)(smem + K2_E2 + (w ^ 2) * 2048) + lane;
    int* ebx = (int*)(smem + K2_EB);
    if (lane == 0) ebx[w] = eb;
#pragma unroll
    for (int k = 0; k < 4; ++k) {
      const f32x2 dr = lo2(Zr, SO + 2 * k), di = lo2(Zi, SO + 2 * k), c = tc[4 * S + k], sn = ts[4 * S + k];
      orr[k] = pk_f16v(dr * c - di * sn); oi[k] = pk_f16v(di * c + dr * sn);
    }
    e2w[0] = orr; e2w[64] = oi;
#pragma unroll
    for (int k = 0; k < 4; ++k) {
      const f32x2 dr = lo2(Zr, KO + 2 * k), di = lo2(Zi, KO + 2 * k), c = tc[4 * S + k], sn = ts[4 * S + k];
      kr[k] = pk_f16v(dr * c - di * sn); ki[k] = pk_f16v(di * c + dr * sn);
    }
    wait_lgkm0();
    raw_bar();
    orr = e2r[0]; oi = e2r[64];
    const int eo = __builtin_amdgcn_readfirstlane(ebx[w ^ 2]);
    const int ebu = __builtin_amdgcn_readfirstlane(eb);
    em = max(ebu, eo);
    if (ebu != eo) {
      const u32 fk = pow2_h2(ebu - em), fo = pow2_h2(eo - em);
#pragma unroll
      for (int i = 0; i < 4; ++i) { kr[i] = pkmul_h(kr[i], fk); ki[i] = pkmul_h(ki[i], fk); orr[i] = pkmul_h(orr[i], fo); oi[i] = pkmul_h(oi[i], fo); }
    }
    Zr = zero16(); Zi = zero16();
    Zr = mf16(Cp[S], kr, Zr); Zi = mf16(Sp[S], kr, Zi);
    Zr = mf16(Np[S], ki, Zr); Zi = mf16(Cp[S], ki, Zi);
    Zr = mf16(Cp[1 - S], orr, Zr); Zi = mf16(Sp[1 - S], orr, Zi);
    Zr = mf16(Np[1 - S], oi, Zr); Zi = mf16(Cp[1 - S], oi, Zi);
  };
  if (sd) finish(IC<1>{}); else finish(IC<0>{});
  const bool prim = (sd == 1) == swp;
  const int ao = prim ? a1 : am, bo = prim ? b1 : bm;
  const u32 cj = sd ? 0x80000000u : 0u;
  if (valid && lane == 0) ecol[ao * 20 + bo] = (unsigned char)em;
  STAMP(X2 - X2_OFF, 2, 7);
  if (!direct) {
    u32x4* su = (u32x4*)(smem + K2_ST) + (((prim ? 0 : 2) + (bo & 1)) * 4) * 64 + lane;
#pragma unroll
    for (int g = 0; g < 4; ++g) {
      u32x4 o = {pk_f16(Zr[4 * g], Zi[4 * g]) ^ cj, pk_f16(Zr[4 * g + 1], Zi[4 * g + 1]) ^ cj,
                 pk_f16(Zr[4 * g + 2], Zi[4 * g + 2]) ^ cj, pk_f16(Zr[4 * g + 3], Zi[4 * g + 3]) ^ cj};
      su[64 * g] = o;
    }
    wait_lgkm0();
    raw_bar();
    STAMP(X2 - X2_OFF, 2, 8);
    const int c2 = b1 >> 1;
#pragma unroll
    for (int set = 0; set < 2; ++set) {
      const int duo = set ? (am * 10 + 9 - c2) : (a1 * 10 + c2);
      const u32x4* sp = (const u32x4*)(smem + K2_ST) + (set * 8 + w) * 64 + lane;
      const u32x4 p0 = sp[0], p1 = sp[256];
      u32x2* dp = (u32x2*)(X2 + (size_t)(duo >> 1) * 16384) + (duo & 1) + 2 * (32 * (8 * w + 4 * h) + r);
#pragma unroll
      for (int i = 0; i < 4; ++i) { u32x2 o = {p0[i], p1[i]}; dp[64 * i] = o; }
    }
  } else if (valid && !(self && sd)) {
    u32* du = (u32*)(X2 + (size_t)(bo >> 2) * 16384) + (bo & 3) + 4 * (128 * h + r);
#pragma unroll
    for (int i = 0; i < 16; ++i) du[128 * crow(i, 0)] = pk_f16(Zr[i], Zi[i]) ^ cj;
  }
}

DI void k2_w1(const u16* __restrict__ wg_, const u16* __restrict__ wu_, char* __restrict__ W1, char* smem, int unit) {
  const int tid = threadIdx.x, lane = tid & 63, w = wave_id();
  const int t = unit >> 3, u = unit & 7;
  u32x2 v[17];
#pragma unroll
  for (int it = 0; it < 17; ++it) {
    const int e = min(tid + 256 * it, 4319);
    const int row2 = e / 27, c8 = e - 27 * row2, mat = row2 >= 80, row = row2 - 80 * mat;
    const u16* base = mat ? wu_ : wg_;
    v[it] = *(const u32x2*)(base + (size_t)(80 * u + row) * 1728 + 108 * t + 4 * c8);
  }
#pragma unroll
  for (int it = 0; it < 17; ++it) {
    const int e = min(tid + 256 * it, 4319);
    const int row2 = e / 27, c8 = e - 27 * row2, mat = row2 >= 80, row = row2 - 80 * mat;
    *(u32x2*)(smem + mat * K2_W1_MAT + row * K2_W1_PITCH + 8 * c8) = v[it];
  }
  if (tid < 160) { u32x2 z = {0u, 0u}; *(u32x2*)(smem + (tid >= 80) * K2_W1_MAT + (tid % 80) * K2_W1_PITCH + 216) = z; }
  __syncthreads();
  const int h2_ = lane >> 5, blk = (lane >> 4) & 1, q4 = (lane & 15) >> 2, p4 = lane & 3;
  const char* lb = smem + blk * K2_W1_MAT + 8 * p4;
  for (int fr = w; fr < 35; fr += 4) {
    const int kk = fr / 7, ft = fr - 7 * kk;
    const int k16 = kk < 4 ? 4 * u + kk : 32 + u;
    const int r0 = kk < 4 ? 20 * kk + 4 * h2_ + q4 : 40 * h2_ + 16 + q4;
    const int r1 = kk < 4 ? r0 + 8 : r0 + 20;
    u32x2 lo = tr_read(lb + r0 * K2_W1_PITCH + 32 * ft), hi = tr_read(lb + r1 * K2_W1_PITCH + 32 * ft);
    const int pos = ft < 3 ? k16 * 3 + ft : 40 * ft + k16;
    u32x4 o = {lo[0], lo[1], hi[0], hi[1]};
    *((u32x4*)(W1 + ((size_t)t * 280 + pos) * 1024) + lane) = o;
  }
}
static_assert(2 * K2_W1_MAT <= K2_LDS, "LDS");

__global__ __launch_bounds__(256, 2) void k2_kernel(const u16* __restrict__ wg_, const u16* __restrict__ wu_, char* __restrict__ ws) {
  __shared__ __attribute__((aligned(16))) char smem[K2_LDS];
  const int bid = blockIdx.x;
  STAMP(ws, 2, 0);
  if (bid < 168) {
    const int lw = (bid & 7) * 21 + (bid >> 3);
    if (lw < NCOLWG) k2_col(ws + X1_OFF, ws + WT_OFF, ws + X2_OFF, (unsigned char*)(ws + EC_OFF), smem, lw);
  } else k2_w1(wg_, wu_, ws + W1_OFF, smem, bid - 168);
  STAMP(ws, 2, 13);
  STAMPE(ws, 2);
}

constexpr int R_BASE = 120 * 1024;
constexpr int R_X2 = R_BASE, R_EC = R_BASE + 10240, R_ST = R_BASE + 12288, R_H = R_ST + 80 * 144;
static_assert(R_H + 10240 <= 160 * 1024, "LDS");
constexpr int PDN = 10, NWF = 12;
constexpr int bnd_slot(int c) { return 40 * c - PDN; }
constexpr int NEPI = 192;
constexpr int epi_slot(int u) { return u < 128 ? 121 + (u * 94) / 128 : 216 + ((u - 128) * 63) / 64; }
constexpr int epi_first(int n) { int u = 0; while (u < NEPI && epi_slot(u) < n) ++u; return u; }
constexpr int gst_slot(int e) { return epi_slot(32 * e + 31) + 1; }
constexpr int SIG_A = 248, POLL_A = 270;
constexpr int SIG_B = 280 + 5 * 2, POLL_B = 280 + 5 * 15, CHECK_B = 280 + 5 * 20;
struct Ev { int slot, kind, a, b; };
constexpr int ev_size(const Ev& e) { return e.kind == 0 ? 4 : e.kind == 1 ? (e.b == 0 ? 4 : 3) : e.kind == 5 ? 5 : 1; }
template <int N> struct Sched {
  Ev ev[N];
  int n;
  constexpr void add(int slot, int kind, int a, int b) {
    int i = n++;
    while (i > 0 && ev[i - 1].slot > slot) { ev[i] = ev[i - 1]; --i; }
    ev[i] = Ev{slot, kind, a, b};
  }
  constexpr int first(int slot) const { int i = 0; while (i < n && ev[i].slot < slot) ++i; return i; }
  constexpr int before(int slot) const { int v = 0; for (int i = 0; i < n; ++i) v += ev[i].slot < slot ? ev_size(ev[i]) : 0; return v; }
  constexpr int end_of(int kind, int a, int b) const {
    int v = 0;
    for (int i = 0; i < n; ++i) { v += ev_size(ev[i]); if (ev[i].kind == kind && ev[i].a == a && ev[i].b == b) return v; }
    return -1000;
  }
};
constexpr Sched<44> make_s4() {
  Sched<44> s{};
  for (int g = 0; g < 10; ++g) s.add(g < 3 ? -1 : 12 * (g - 3) + 1, 0, g, 0);
  const int c0[7] = {80, 100, 124, 151, 181, 205, 232}, dp[7] = {6, 8, 9, 9, 8, 7, 6};
  for (int c = 0; c < 7; ++c) for (int p = 0; p < 3; ++p) s.add(c0[c] + dp[c] * p, 1, 3 + c, p);
  for (int e = 0; e < 6; ++e) s.add(gst_slot(e), 3, e, 0);
  s.add(POLL_A, 2, 0, 0);
  return s;
}
constexpr Sched<76> make_s5() {
  Sched<76> s{};
  for (int k = 0; k < 56; ++k) s.add(k < 12 ? -1 : 280 + 5 * (k - 12) + 1, 4, k, 0);
  for (int c = 0; c < 4; ++c) for (int p = 0; p < 3; ++p) s.add(280 + 5 * (4 + 8 * c + 2 * p) + 3, 1, 10 + c, p);
  s.add(POLL_B, 2, 0, 0);
  s.add(280 + 5 * 45, 5, 0, 0);
  return s;
}
constexpr int cap63(int v) { return v < 63 ? v : 63; }
constexpr Sched<44> S4 = make_s4();
constexpr Sched<76> S5 = make_s5();

__global__ __launch_bounds__(256, 1) void k3_kernel(const u16* __restrict__ x, char* __restrict__ ws, u16* __restrict__ out) {
  __shared__ __attribute__((aligned(16))) char smem[160 * 1024];
  const int tid = threadIdx.x, lane = tid & 63, w = wave_id(), r = lane & 31, h = lane >> 5;
  const int bid = blockIdx.x, xcd = bid & 7, idx = bid >> 3;
  const int rb = 4 * (xcd & 3) + (idx & 3), t = 8 * (xcd >> 2) + (idx >> 2);
  const int mb = 2 * rb + (xcd >> 2), nb = idx >> 2;
  const u32 lds0 = lds_addr(smem), voff = lane * 16;
  const char* w1g = ws + W1_OFF + (size_t)t * (280 * 1024);
  const char* w2g = ws + W2_OFF + (size_t)nb * (280 * 1024);
  auto chunk_part = [&](auto C, auto P) {
    constexpr int c = decltype(C)::v, p = decltype(P)::v, o = p == 0 ? 0 : (p == 1 ? 4096 : 7168);
    const char* g = (c < 7 ? w1g + c * 40960 : w2g + (c - 7) * 40960) + w * 10240 + o;
    dma<(p == 0 ? 4 : 3)>(voff, g, lds0 + (c & 3) * 40960 + w * 10240 + o);
  };
  STAMP(ws, 3, 0);
  {
    const int wg = 16 * rb + t, m = 4 * wg + w;
    const char* X2 = ws + X2_OFF;
#pragma unroll
    for (int n = 0; n < 3; ++n) {
      const int pc = w + 4 * n;
      const u32 vx = (u32)(pc * 16 + (lane >> 2)) * 16384u + (u32)(wg * 64 + (lane & 3) * 16);
      const u32 ve = (u32)X2_SZ + (pc == 10 ? (u32)min(lane, 39) * 16u : 0u);
      dma<1>(pc < 10 ? vx : ve, X2, lds0 + R_X2 + pc * 1024);
    }
    u32x2 xr[6];
    {
      const char* src = (const char*)x + (size_t)m * 2560;
      const u32 vo = 40 * r + 8 * h, v2 = 40 * r + 32;
      asm volatile("global_load_dwordx2 %0, %6, %8\n\tglobal_load_dwordx2 %1, %6, %8 offset:16\n\tglobal_load_dwordx2 %2, %7, %8\n\t"
                   "global_load_dwordx2 %3, %6, %8 offset:1280\n\tglobal_load_dwordx2 %4, %6, %8 offset:1296\n\t"
                   "global_load_dwordx2 %5, %7, %8 offset:1280"
                   : "=&v"(xr[0]), "=&v"(xr[1]), "=&v"(xr[2]), "=&v"(xr[3]), "=&v"(xr[4]), "=&v"(xr[5])
                   : "v"(vo), "v"(v2), "s"(src) : "memory");
    }
    u32x4 BR[3], BI[3], Cp[2], Sp[2], Np[2];
    f32x2 tc[8], ts[8];
    {
      float c[16], s[16];
      dft32_consts(c, s, r, h);
      pack_csn(c, s, Cp, Sp, Np);
#pragma unroll
      for (int i = 0; i < 2; ++i) { pin(Cp[i]); pin(Sp[i]); pin(Np[i]); }
      const float rf = (float)r * (1.f / 640.f), r4 = rf * (float)(4 * h);
      static_for<0, 4>([&](auto Q) {
        constexpr int q = decltype(Q)::v;
#pragma unroll
        for (int i = 4 * q; i < 4 * q + 4; ++i) {
          const float rev = rf * (float)crow(i, 0) + r4;
          tc[i >> 1][i & 1] = cosr(rev); ts[i >> 1][i & 1] = sinr(rev);
          if (i < 12) {
            const float rev2 = rev * 32.f, c2 = cosr(rev2), s2 = sinr(rev2);
            const u32 br = pk_f16(c2, -s2), bi = pk_f16(s2, c2);
            BR[q][i & 3] = (i >= 8 && h) ? 0u : br; BI[q][i & 3] = (i >= 8 && h) ? 0u : bi;
          }
        }
        if constexpr (q < 3) { pin(BR[q]); pin(BI[q]); }
        pin(tc[2 * q]); pin(ts[2 * q]); pin(tc[2 * q + 1]); pin(ts[2 * q + 1]);
      });
    }
    wait_vm<6>();
    raw_bar();
    STAMP(ws, 3, 1);
    u32x4 a[3];
    u32 ec[3];
#pragma unroll
    for (int ks = 0; ks < 3; ++ks) {
      const int c = (ks == 2) ? 4 : 2 * ks + h;
      a[ks] = *(const u32x4*)(smem + R_X2 + (r * 5 + c) * 64 + w * 16);
      ec[ks] = *(const u32*)(smem + R_EC + (r * 5 + c) * 4);
    }
    int ex[12];
    u32 em = 0;
#pragma unroll
    for (int i = 0; i < 12; ++i) { ex[i] = (ec[i >> 2] >> (8 * (i & 3))) & 0xff; em = umax(em, (u32)ex[i]); }
    const int E = (int)max64(em);
#pragma unroll
    for (int i = 0; i < 12; ++i) a[i >> 2][i & 3] = pkmul_h(a[i >> 2][i & 3], pow2_h2(ex[i] - (E + 5)));
    const float fs = __builtin_amdgcn_ldexpf(1.f / 1280.f, E - 127 - 9);
    f32x16 Tr = zero16(), Ti = zero16();
#pragma unroll
    for (int ks = 0; ks < 3; ++ks) { Tr = mf16(a[ks], BR[ks], Tr); Ti = mf16(a[ks], BI[ks], Ti); }
    u32x4 P[4];
#pragma unroll
    for (int k = 0; k < 8; ++k) {
      const f32x2 dr = lo2(Tr, 2 * k), di = lo2(Ti, 2 * k);
      P[k >> 2][k & 3] = pk_f16v(dr * tc[k] - di * ts[k]);
      P[2 + (k >> 2)][k & 3] = pk_f16v(di * tc[k] + dr * ts[k]);
    }
    f32x16 Vr = zero16(), Vi = zero16();
#pragma unroll
    for (int s = 0; s < 2; ++s) {
      Vr = mf16(P[s], Cp[s], Vr); Vi = mf16(P[s], Sp[s], Vi);
      Vr = mf16(P[2 + s], Np[s], Vr); Vi = mf16(P[2 + s], Cp[s], Vi);
    }
    asm volatile("s_waitcnt vmcnt(0)" : "+v"(xr[0]), "+v"(xr[1]), "+v"(xr[2]), "+v"(xr[3]), "+v"(xr[4]), "+v"(xr[5])::"memory");
    STAMP(ws, 3, 2);
    f32x2 hv[2][6];
    u32 hp[2][6];
    float mean[2], var[2];
#pragma unroll
    for (int rr = 0; rr < 2; ++rr) {
      f32x2 sm = {0.f, 0.f};
#pragma unroll
      for (int k = 0; k < 6; ++k) {
        const f32x2 y = (rr ? lo2(Vi, 2 * k) : lo2(Vr, 2 * k)) * fs;
        const u32 yb = pk_bf16(y[0], y[1]);
        const u32 xx = xr[3 * rr + (k >> 1)][k & 1];
        const f32x2 hs = f32x2{bf_lo(xx), bf_hi(xx)} + f32x2{bf_lo(yb), bf_hi(yb)};
        u32 hh = pk_bf16(hs[0], hs[1]);
        if (k >= 4) hh = h ? 0u : hh;
        hp[rr][k] = hh;
        hv[rr][k] = f32x2{bf_lo(hh), bf_hi(hh)};
        sm += hv[rr][k];
      }
      mean[rr] = sm[0] + sm[1];
    }
#pragma unroll
    for (int rr = 0; rr < 2; ++rr) {
      char* hl = smem + R_H + (2 * w + rr) * 1280 + 40 * r;
      *(u32x2*)(hl + 8 * h) = u32x2{hp[rr][0], hp[rr][1]};
      *(u32x2*)(hl + 8 * h + 16) = u32x2{hp[rr][2], hp[rr][3]};
      if (h == 0) *(u32x2*)(hl + 32) = u32x2{hp[rr][4], hp[rr][5]};
    }
#pragma unroll
    for (int rr = 0; rr < 2; ++rr) mean[rr] = sum64(mean[rr]) * (1.f / 640.f);
#pragma unroll
    for (int rr = 0; rr < 2; ++rr) {
      f32x2 q2 = {0.f, 0.f}, q3 = {0.f, 0.f};
#pragma unroll
      for (int k = 0; k < 4; ++k) { const f32x2 d = hv[rr][k] - mean[rr]; q2 += d * d; }
#pragma unroll
      for (int k = 4; k < 6; ++k) { const f32x2 d = hv[rr][k] - mean[rr]; q3 += d * d; }
      var[rr] = q2[0] + q2[1] + (h ? 0.f : q3[0] + q3[1]);
    }
#pragma unroll
    for (int rr = 0; rr < 2; ++rr) var[rr] = sum64(var[rr]) * (1.f / 640.f);
#pragma unroll
    for (int rr = 0; rr < 2; ++rr) {
      float mbn, inv;
      ln_scalars(mean[rr], var[rr], mbn, inv);
      u32 o[6];
#pragma unroll
      for (int k = 0; k < 6; ++k) {
        const f32x2 d0 = hv[rr][k] - mbn;
        const u32 d = pk_bf16(d0[0], d0[1]);
        const f32x2 n0 = f32x2{bf_lo(d), bf_hi(d)} * inv;
        o[k] = pk_bf16(n0[0], n0[1]);
      }
      const int rowl = 2 * w + rr;
      u32x4 o4 = {o[0], o[1], o[2], o[3]};
      *(u32x4*)(smem + R_ST + (h * 32 + r) * 144 + rowl * 16) = o4;
      if (h == 0) { u32x2 o2 = {o[4], o[5]}; *(u32x2*)(smem + R_ST + (64 + (r >> 1)) * 144 + rowl * 16 + (r & 1) * 8) = o2; }
    }
    wait_lgkm0();
    raw_bar();
    STAMP(ws, 3, 3);
    const int row0 = 8 * wg;
    const char* ab = ws + ACT_OFF + (size_t)(row0 >> 5) * (40 * 1024) + (row0 & 31) * 16;
#pragma unroll
    for (int it = 0; it < 3; ++it) {
      const int gi = (tid >> 3) + 32 * it;
      if (gi < 80) {
        const int L = gi < 64 ? (gi & 1) * 32 + (gi >> 1) : gi;
        st16_dev(gi * 512 + (tid & 7) * 16, ab, *(const u32x4*)(smem + R_ST + L * 144 + (tid & 7) * 16));
      }
    }
  }
  STAMP(ws, 3, 4);
  auto pre1 = [&] {
    const char* hb = ws + H_OFF + (size_t)(16 * rb + t) * 10240;
#pragma unroll
    for (int i = 0; i < 4; ++i) {
      const int c = tid - 64 + 192 * i;
      if (c < 640) st16_dev(c * 16, hb, *(const u32x4*)(smem + R_H + c * 16));
    }
#pragma unroll
    for (int g = 0; g < 10; ++g) dma<4>(voff, w1g + (3 * g + w - 1) * 4096, lds0 + (3 * g + w - 1) * 4096);
  };
  publish_and_wait((u32*)(ws + CNT_OFF + rb * 256), tid, w, pre1);
  STAMP(ws, 3, 5);

  u32 lb[3];
#pragma unroll
  for (int i = 0; i < 3; ++i) { lb[i] = lane * 16 + i * 65536; pin(lb[i]); }
  u32x4 wf[NWF];
  u32 pollv;
  u32* cntA = (u32*)(ws + CNT_OFF + (16 + rb) * 256);
  u32* cntB = (u32*)(ws + CNT_OFF + (32 + rb) * 256);
  auto rdw = [&](auto N) {
    constexpr int n = decltype(N)::v, o = (n % 160) * 1024;
    if constexpr (n < 560) wf[n % NWF] = *(const u32x4*)(smem + lb[o >> 16] + (o & 65535));
  };
  auto boundary = [&](auto WN) {
    wait_lgkm0();
    wait_vm<decltype(WN)::v>();
    raw_bar();
  };
  {
    u32x4 act[40];
    const char* actg = ws + ACT_OFF + (size_t)(4 * rb + w) * (40 * 1024);
    f32x16 acc[7];
    u32x4 gout;
    float ez[2][2];
    const int m = 128 * rb + 32 * w + r;
    const char* gdst = ws + G_OFF + (size_t)((2 * rb + (w >> 1)) * 56) * 4096 + (w & 1) * 2048;
    const u32 gvo = (r >> 4) * 1024 + (r & 15) * 16 + h * 256;
    float k1 = -2.3022081983f;
    pin(k1);
    auto epi = [&](auto U) {
      constexpr int u = decltype(U)::v, z = u & 1, st = (u >> 1) & 7, pr = 2 * (u >> 4) + z, ft = pr / 4, p = pr % 4;
#pragma unroll
      for (int e = 0; e < 2; ++e) {
        const float g = acc[ft][2 * p + e];
        if constexpr (st == 0) ez[z][e] = vmul(g, g);
        if constexpr (st == 1) ez[z][e] = vpoly(ez[z][e], k1);
        if constexpr (st == 2) ez[z][e] = vmul(ez[z][e], g);
        if constexpr (st == 3) ez[z][e] = vexp(ez[z][e]);
        if constexpr (st == 4) { ez[z][e] = vadd1(ez[z][e]); }
        if constexpr (st == 5) ez[z][e] = vrcp(ez[z][e]);
        if constexpr (st == 6) ez[z][e] = vmul(g, ez[z][e]);
        if constexpr (st == 7) ez[z][e] = vmul(ez[z][e], acc[ft][8 + 2 * p + e]);
      }
      if constexpr (st == 7) gout[p] = pk_bf16(ez[z][0], ez[z][1]);
    };
    auto gstore = [&](int ft) {
      const int kk = ft < 4 ? 2 * t + (ft >> 1) : (ft < 6 ? 32 + t : 48 + (t >> 1)), g2 = ft < 6 ? ft & 1 : t & 1;
      st16_dev(gvo, gdst + kk * 4096 + g2 * 512, gout);
    };
    auto events = [&](auto N) {
      static_for<S4.first(decltype(N)::v), S4.first(decltype(N)::v + 1)>([&](auto I) {
        constexpr Ev e = S4.ev[decltype(I)::v];
        {
          if constexpr (e.kind == 0) ld4_dev(act[4 * e.a], act[4 * e.a + 1], act[4 * e.a + 2], act[4 * e.a + 3], voff, actg + e.a * 4096);
          if constexpr (e.kind == 1) chunk_part(IC<e.a>{}, IC<e.b>{});
          if constexpr (e.kind == 3) gstore(e.a);
          if constexpr (e.kind == 2) poll_issue(pollv, cntA);
        }
      });
    };
    events(IC<-1>{});
    boundary(IC<24 + 12>{});
    static_for<0, PDN>(rdw);
    static_for<0, 280>([&](auto N) {
      constexpr int n = decltype(N)::v;
      constexpr int ft = n < 120 ? n % 3 : n / 40, k16 = n < 120 ? n / 3 : n % 40;
      if constexpr (n < 120 && n % 6 == 0)
        wait_a2<S4.before(n) - (S4.end_of(0, k16 / 4, 0) - 2 + k16 % 4)>(act[k16], act[k16 + 1]);
      mfma32<k16 == 0>(acc[ft], wf[n % NWF], act[k16]);
      __builtin_amdgcn_sched_barrier(0);
      if constexpr ((n + PDN) % 40 == 0 && n + PDN < 280) {
        constexpr int c = (n + PDN) / 40;
        boundary(IC<cap63(c < 3 ? S4.before(n) + 12 * (2 - c) : S4.before(n) - S4.end_of(1, c, 2))>{});
      }
      if constexpr (n == SIG_A) {
        wait_vm<S4.before(n) - S4.end_of(3, 3, 0)>();
        raw_bar();
        signal(cntA, tid);
      }
      if constexpr (n + PDN < 280) rdw(IC<n + PDN>{});
      static_for<0, 2>([&](auto K) {
        constexpr int u = epi_first(n) + decltype(K)::v;
        if constexpr (u < NEPI) { if constexpr (epi_slot(u) == n) epi(IC<u>{}); }
      });
      events(N);
      __builtin_amdgcn_sched_barrier(0);
    });
    STAMP(ws, 3, 9);
    asm volatile("s_nop 7\n\ts_nop 7" : "+v"(acc[6]));
#pragma unroll
    for (int p = 0; p < 4; ++p) {
      const f32x2 g = lo2(acc[6], 2 * p), u = lo2(acc[6], 8 + 2 * p);
      const f32x2 z = g * (g * g * -0.1029432397f + -2.3022081983f);
      const f32x2 den = f32x2{__builtin_amdgcn_exp2f(z[0]), __builtin_amdgcn_exp2f(z[1])} + 1.f;
      const f32x2 ov = (g * f32x2{__builtin_amdgcn_rcpf(den[0]), __builtin_amdgcn_rcpf(den[1])}) * u;
      gout[p] = pk_bf16(ov[0], ov[1]);
    }
    gstore(6);
  }
  STAMP(ws, 3, 10);
    poll_check<S4.before(1000) + 1 - S4.end_of(2, 0, 0)>(pollv, cntA);
  boundary(IC<S4.before(1000) + 1 - S4.end_of(1, 7, 2)>{});
  STAMP(ws, 3, 11);
  {
    u32x4 gf[56];
    u32x2 hv[5];
    const int mrow = 64 * mb + 16 * w + (lane & 15), ncol = 80 * nb + 4 * (lane >> 4);
    const char* gg = ws + G_OFF + (size_t)(mb * 56) * 4096 + w * 1024;
    auto events = [&](auto N) {
      static_for<S5.first(decltype(N)::v), S5.first(decltype(N)::v + 1)>([&](auto I) {
        constexpr Ev e = S5.ev[decltype(I)::v];
        {
          if constexpr (e.kind == 4) ld1_dev(gf[e.a], voff, gg + e.a * 4096);
          if constexpr (e.kind == 1) chunk_part(IC<e.a>{}, IC<e.b>{});
          if constexpr (e.kind == 2) poll_issue(pollv, cntB);
          if constexpr (e.kind == 5) ldh_dev(hv, (u32)(mrow * 640 + ncol) * 2u, ws + H_OFF);
        }
      });
    };
    events(IC<-1>{});
    f32x4 acc[5];
    static_for<280, 280 + PDN>(rdw);
    static_for<280, 560>([&](auto N) {
      constexpr int n = decltype(N)::v, k = (n - 280) / 5, nt = (n - 280) % 5;
      if constexpr (nt == 0 && k % 2 == 0) wait_a2<S5.before(n) - S5.end_of(4, k + 1, 0)>(gf[k], gf[k + 1]);
      mfma16<k == 0>(acc[nt], wf[n % NWF], gf[k]);
      __builtin_amdgcn_sched_barrier(0);
      if constexpr ((n + PDN) % 40 == 0 && n + PDN < 560) {
        constexpr int c = (n + PDN) / 40;
        boundary(IC<cap63(c < 10 ? 63 : S5.before(n) - S5.end_of(1, c, 2))>{});
      }
      if constexpr (n == SIG_B) {
        raw_bar();
        signal(cntB, tid);
      }
      if constexpr (n == CHECK_B) poll_check<S5.before(n) - S5.end_of(2, 0, 0)>(pollv, cntB);
      rdw(IC<n + PDN>{});
      events(N);
      __builtin_amdgcn_sched_barrier(0);
    });
    STAMP(ws, 3, 12);
    asm volatile("s_waitcnt vmcnt(0)\n\ts_nop 7\n\ts_nop 7"
                 : "+v"(hv[0]), "+v"(hv[1]), "+v"(hv[2]), "+v"(hv[3]), "+v"(hv[4]), "+v"(acc[0]), "+v"(acc[1]), "+v"(acc[2]),
                   "+v"(acc[3]), "+v"(acc[4])::"memory");
    u16* op = out + (size_t)mrow * 640 + ncol;
#pragma unroll
    for (int j = 0; j < 5; ++j) {
      const u32 o0 = pk_bf16(acc[j][0], acc[j][1]), o1 = pk_bf16(acc[j][2], acc[j][3]);
      u32x2 o = {pk_bf16(bf_lo(hv[j][0]) + bf_lo(o0), bf_hi(hv[j][0]) + bf_hi(o0)),
                 pk_bf16(bf_lo(hv[j][1]) + bf_lo(o1), bf_hi(hv[j][1]) + bf_hi(o1))};
      *(u32x2*)(op + 16 * j) = o;
    }
  }
  STAMP(ws, 3, 13);
  STAMPE(ws, 3);
}

extern "C" void kernel_launch(void* const* d_in, const int* in_sizes, int n_in, void* d_out, int out_size, void* d_ws,
                              size_t ws_size, hipStream_t stream) {
  const u16* x = (const u16*)d_in[0];
  const u16* wr = (const u16*)d_in[1];
  const u16* wi = (const u16*)d_in[2];
  const u16* wg = (const u16*)d_in[4];
  const u16* wu = (const u16*)d_in[5];
  const u16* wd = (const u16*)d_in[6];
  char* ws = (char*)d_ws;
  k1_kernel<<<K1_GRID, 256, 0, stream>>>(x, wr, wi, wd, ws);
  k2_kernel<<<K2_GRID, 256, 0, stream>>>(wg, wu, ws);
  k3_kernel<<<256, 256, 0, stream>>>(x, ws, (u16*)d_out);
}
```

```cpp
#include <hip/hip_runtime.h>
#include <hip/hip_bf16.h>
#include <stdint.h>

#define DI __device__ __forceinline__
typedef unsigned short u16;
typedef uint32_t u32;
typedef _Float16 f16;
typedef f16 f16x2 __attribute__((ext_vector_type(2)));
typedef f16 f16x4 __attribute__((ext_vector_type(4)));
typedef f16 f16x8 __attribute__((ext_vector_type(8)));
typedef __bf16 bf16x2 __attribute__((ext_vector_type(2)));
typedef __bf16 bf16x8 __attribute__((ext_vector_type(8)));
typedef short s16x4 __attribute__((ext_vector_type(4)));
typedef float f32x2 __attribute__((ext_vector_type(2)));
typedef float f32x4 __attribute__((ext_vector_type(4)));
typedef float f32x16 __attribute__((ext_vector_type(16)));
typedef u32 u32x2 __attribute__((ext_vector_type(2)));
typedef u32 u32x4 __attribute__((ext_vector_type(4)));
typedef int i32x4 __attribute__((ext_vector_type(4)));
#define LAS __attribute__((address_space(3)))

constexpr int NCOLWG = 162, NSLOT = 4 * NCOLWG;
constexpr size_t X1_OFF = 0;
constexpr size_t X1_SZ = 256ull * NSLOT * 16;
constexpr size_t WT_OFF = X1_OFF + X1_SZ;
constexpr size_t WT_SZ = 323ull * 8192;
constexpr size_t X2_OFF = WT_OFF + WT_SZ;
constexpr size_t X2_SZ = 160ull * 16384;
constexpr size_t EC_OFF = X2_OFF + X2_SZ;
constexpr size_t EC_SZ = 4096;
constexpr size_t H_OFF = EC_OFF + EC_SZ;
constexpr size_t H_SZ = 2048ull * 640 * 2;
constexpr size_t ACT_OFF = H_OFF + H_SZ;
constexpr size_t ACT_SZ = 64ull * 40 * 1024;
constexpr size_t W1_OFF = ACT_OFF + ACT_SZ;
constexpr size_t W1_SZ = 16ull * 280 * 1024;
constexpr size_t W2_OFF = W1_OFF + W1_SZ;
constexpr size_t W2_SZ = 8ull * 56 * 5 * 1024;
constexpr size_t G_OFF = W2_OFF + W2_SZ;
constexpr size_t G_SZ = 32ull * 56 * 4 * 1024;
constexpr size_t CNT_OFF = G_OFF + G_SZ;

#define STAMPN(ws_, k_, i_) do {} while (0)
#define STAMPF(ws_, k_, i_) do {} while (0)
#define STAMP(ws_, k_, i_) do {} while (0)
#define STAMPE(ws_, k_) do {} while (0)
DI float asf(u32 u) { return __builtin_bit_cast(float, u); }
DI u32 asu(float f) { return __builtin_bit_cast(u32, f); }
DI u32 pk_bf16(float a, float b) { f32x2 v = {a, b}; return __builtin_bit_cast(u32, __builtin_convertvector(v, bf16x2)); }
DI u32 pk_f16(float a, float b) { f32x2 v = {a, b}; return __builtin_bit_cast(u32, __builtin_convertvector(v, f16x2)); }
DI u32 pk_f16v(f32x2 v) { return __builtin_bit_cast(u32, __builtin_convertvector(v, f16x2)); }
DI float bf_lo(u32 u) { return asf(u << 16); }
DI float bf_hi(u32 u) { return asf(u & 0xffff0000u); }
DI float bfr(float a) { return bf_lo(pk_bf16(a, a)); }
DI float cosr(float rev) { return __builtin_amdgcn_cosf(rev); }
DI float sinr(float rev) { return __builtin_amdgcn_sinf(rev); }
constexpr __host__ __device__ int crow(int reg, int h) { return (reg & 3) + 8 * (reg >> 2) + 4 * h; }
DI u32 umax(u32 a, u32 b) { return a > b ? a : b; }

template <int CTRL> DI u32 dppu(u32 v) { return (u32)__builtin_amdgcn_update_dpp(0, (int)v, CTRL, 0xf, 0xf, true); }
template <int CTRL> DI float dpp(float v) { return asf(dppu<CTRL>(asu(v))); }
DI float sum32(float v) {
  v += dpp<0xB1>(v); v += dpp<0x4E>(v); v += dpp<0x141>(v); v += dpp<0x140>(v);
  auto r = __builtin_amdgcn_permlane16_swap(asu(v), asu(v), false, false);
  return asf(r[0]) + asf(r[1]);
}
DI float sum64(float v) {
  v = sum32(v);
  auto r = __builtin_amdgcn_permlane32_swap(asu(v), asu(v), false, false);
  return asf(r[0]) + asf(r[1]);
}
DI u32 max64(u32 v) {
  v = umax(v, dppu<0xB1>(v)); v = umax(v, dppu<0x4E>(v)); v = umax(v, dppu<0x141>(v)); v = umax(v, dppu<0x140>(v));
  auto r = __builtin_amdgcn_permlane16_swap(v, v, false, false);
  v = umax(r[0], r[1]);
  auto s = __builtin_amdgcn_permlane32_swap(v, v, false, false);
  return umax(s[0], s[1]);
}
DI f16x8 asf16x8(u32x4 v) { return __builtin_bit_cast(f16x8, v); }
DI f32x16 mf16(u32x4 a, u32x4 b, f32x16 c) { return __builtin_amdgcn_mfma_f32_32x32x16_f16(asf16x8(a), asf16x8(b), c, 0, 0, 0); }
DI f32x16 mb32(u32x4 a, u32x4 b, f32x16 c) {
  return __builtin_amdgcn_mfma_f32_32x32x16_bf16(__builtin_bit_cast(bf16x8, a), __builtin_bit_cast(bf16x8, b), c, 0, 0, 0);
}
DI f32x4 mb16(u32x4 a, u32x4 b, f32x4 c) {
  return __builtin_amdgcn_mfma_f32_16x16x32_bf16(__builtin_bit_cast(bf16x8, a), __builtin_bit_cast(bf16x8, b), c, 0, 0, 0);
}
DI u32x2 tr_read(const char* p) {
  s16x4 r = __builtin_amdgcn_ds_read_tr16_b64_v4i16((LAS s16x4*)(p));
  return __builtin_bit_cast(u32x2, r);
}
DI f32x16 zero16() { f32x16 z; for (int i = 0; i < 16; ++i) z[i] = 0.f; return z; }
DI int wave_id() { return __builtin_amdgcn_readfirstlane(threadIdx.x >> 6); }
DI f32x2 lo2(const f32x16& v, int i) { return f32x2{v[i], v[i + 1]}; }
DI f32x2 pr2(const f32x4& v, int i) { return f32x2{v[i], v[i + 1]}; }
DI u32 pkmul_h(u32 a, u32 b) {
  const f16x2 r = __builtin_bit_cast(f16x2, a) * __builtin_bit_cast(f16x2, b);
  return __builtin_bit_cast(u32, r);
}
DI u32 pow2_h2(int d) {
  const float s = __builtin_amdgcn_ldexpf(1.f, d);
  return pk_f16(s, s);
}
template <class T> DI void pin(T& v) { asm volatile("" : "+v"(v)); }

DI void ln_scalars(float mean, float var, float& mb, float& inv) {
  mb = bfr(mean);
  float vb = bfr(var);
  float t = bfr(vb + 1e-5f);
  float den = bfr(__builtin_amdgcn_sqrtf(t));
  inv = __builtin_amdgcn_rcpf(den);
}

template <int N> struct IC { static constexpr int v = N; };
template <int I, int N, class F> DI void static_for(F&& f) {
  if constexpr (I < N) { f(IC<I>{}); static_for<I + 1, N>(f); }
}
template <int N> DI void wait_vm() { asm volatile("s_waitcnt vmcnt(%0)" ::"n"(N) : "memory"); }
DI void wait_lgkm0() { asm volatile("s_waitcnt lgkmcnt(0)" ::: "memory"); }
DI void raw_bar() { asm volatile("s_barrier" ::: "memory"); }
DI u32 lds_addr(const char* p) { return (u32)(size_t)(LAS const char*)p; }

template <int N> DI void dma(u32 voff, const void* g, u32 lds) {
  if constexpr (N == 4)
    asm volatile("s_mov_b32 m0, %2\n\ts_nop 0\n\tglobal_load_lds_dwordx4 %0, %1\n\t"
                 "global_load_lds_dwordx4 %0, %1 offset:1024\n\tglobal_load_lds_dwordx4 %0, %1 offset:2048\n\t"
                 "global_load_lds_dwordx4 %0, %1 offset:3072" ::"v"(voff), "s"(g), "s"(lds) : "memory");
  else if constexpr (N == 3)
    asm volatile("s_mov_b32 m0, %2\n\ts_nop 0\n\tglobal_load_lds_dwordx4 %0, %1\n\t"
                 "global_load_lds_dwordx4 %0, %1 offset:1024\n\tglobal_load_lds_dwordx4 %0, %1 offset:2048" ::"v"(voff),
                 "s"(g), "s"(lds) : "memory");
  else if constexpr (N == 2)
    asm volatile("s_mov_b32 m0, %2\n\ts_nop 0\n\tglobal_load_lds_dwordx4 %0, %1\n\t"
                 "global_load_lds_dwordx4 %0, %1 offset:1024" ::"v"(voff), "s"(g), "s"(lds) : "memory");
  else
    asm volatile("s_mov_b32 m0, %2\n\ts_nop 0\n\tglobal_load_lds_dwordx4 %0, %1" ::"v"(voff), "s"(g), "s"(lds) : "memory");
}
DI void st16_dev(u32 voff, const void* g, u32x4 v) {
  asm volatile("global_store_dwordx4 %0, %1, %2 sc1\n\ts_nop 1" ::"v"(voff), "v"(v), "s"(g) : "memory");
}
DI void st8_dev(u32 voff, const void* g, u32x2 v) {
  asm volatile("global_store_dwordx2 %0, %1, %2 sc1" ::"v"(voff), "v"(v), "s"(g) : "memory");
}
DI void ld4_dev(u32x4& a0, u32x4& a1, u32x4& a2, u32x4& a3, u32 voff, const void* g) {
  asm volatile("global_load_dwordx4 %0, %4, %5 sc1\n\tglobal_load_dwordx4 %1, %4, %5 offset:1024 sc1\n\t"
               "global_load_dwordx4 %2, %4, %5 offset:2048 sc1\n\tglobal_load_dwordx4 %3, %4, %5 offset:3072 sc1"
               : "=&a"(a0), "=&a"(a1), "=&a"(a2), "=&a"(a3) : "v"(voff), "s"(g) : "memory");
}
DI void ld2_dev(u32x4& a0, u32x4& a1, u32 v0, u32 v1, const void* g) {
  asm volatile("global_load_dwordx4 %0, %2, %4 sc1\n\tglobal_load_dwordx4 %1, %3, %4 sc1"
               : "=&a"(a0), "=&a"(a1) : "v"(v0), "v"(v1), "s"(g) : "memory");
}
DI void ldh_dev(u32x2 (&hv)[5], u32 vo, const void* g) {
  asm volatile("global_load_dwordx2 %0, %5, %6 sc1\n\tglobal_load_dwordx2 %1, %5, %6 offset:32 sc1\n\t"
               "global_load_dwordx2 %2, %5, %6 offset:64 sc1\n\tglobal_load_dwordx2 %3, %5, %6 offset:96 sc1\n\t"
               "global_load_dwordx2 %4, %5, %6 offset:128 sc1"
               : "=&v"(hv[0]), "=&v"(hv[1]), "=&v"(hv[2]), "=&v"(hv[3]), "=&v"(hv[4]) : "v"(vo), "s"(g) : "memory");
}
template <int N> DI void wait_a2(u32x4& a0, u32x4& a1) {
  asm volatile("s_waitcnt vmcnt(%2)" : "+a"(a0), "+a"(a1) : "n"(N) : "memory");
}
DI float vmul(float a, float b) { float d; asm volatile("v_mul_f32 %0, %1, %2" : "=v"(d) : "v"(a), "v"(b)); return d; }
DI float vpoly(float z, float k1) { float d; asm volatile("v_fmamk_f32 %0, %1, 0xbdd2d3e8, %2" : "=v"(d) : "v"(z), "v"(k1)); return d; }
DI float vadd1(float a) { float d; asm volatile("v_add_f32 %0, 1.0, %1" : "=v"(d) : "v"(a)); return d; }
DI float vexp(float a) { float d; asm volatile("v_exp_f32 %0, %1" : "=v"(d) : "v"(a)); return d; }
DI float vrcp(float a) { float d; asm volatile("v_rcp_f32 %0, %1" : "=v"(d) : "v"(a)); return d; }
template <bool FIRST> DI void mfma32(f32x16& d, const u32x4& a, const u32x4& b) {
  if constexpr (FIRST) asm volatile("v_mfma_f32_32x32x16_bf16 %0, %1, %2, 0" : "=&v"(d) : "v"(a), "a"(b));
  else asm volatile("v_mfma_f32_32x32x16_bf16 %0, %1, %2, %0" : "+v"(d) : "v"(a), "a"(b));
}
template <bool FIRST> DI void mfma16(f32x4& d, const u32x4& a, const u32x4& b) {
  if constexpr (FIRST) asm volatile("v_mfma_f32_16x16x32_bf16 %0, %1, %2, 0" : "=&v"(d) : "v"(a), "a"(b));
  else asm volatile("v_mfma_f32_16x16x32_bf16 %0, %1, %2, %0" : "+v"(d) : "v"(a), "a"(b));
}
DI void signal(u32* cnt, int tid) {
  if (tid == 0) __hip_atomic_fetch_add(cnt, 1u, __ATOMIC_RELAXED, __HIP_MEMORY_SCOPE_AGENT);
}
DI void poll_issue(u32& v, const u32* cnt) {
  asm volatile("global_load_dword %0, %1, %2 sc1" : "=&v"(v) : "v"(0), "s"(cnt) : "memory");
}
template <int N> DI void poll_check(u32& v, u32* cnt) {
  asm volatile("s_waitcnt vmcnt(%1)" : "+v"(v) : "n"(N) : "memory");
  if (__builtin_amdgcn_readfirstlane(v) < 16u) {
    for (int it = 0; it < (1 << 20); ++it) {
      if (__builtin_amdgcn_readfirstlane(__hip_atomic_load(cnt, __ATOMIC_RELAXED, __HIP_MEMORY_SCOPE_AGENT)) >= 16u) break;
      __builtin_amdgcn_s_sleep(1);
    }
  }
  asm volatile("" ::: "memory");
}
DI void tstamp(unsigned long long& t) { asm volatile("s_memtime %0\n\ts_waitcnt lgkmcnt(0)" : "=s"(t)::"memory"); }
#define PW_STAMP
#define PW_STAMP2
template <class F> DI void publish_and_wait(u32* cnt, int tid, int w, F&& idle) {
  asm volatile("s_waitcnt vmcnt(0) lgkmcnt(0)" ::: "memory");
  raw_bar();
  PW_STAMP2;
  if (w == 0) {
    if (tid == 0) __hip_atomic_fetch_add(cnt, 1u, __ATOMIC_RELAXED, __HIP_MEMORY_SCOPE_AGENT);
    for (int it = 0; it < (1 << 20); ++it) {
      const u32 v = __builtin_amdgcn_readfirstlane(__hip_atomic_load(cnt, __ATOMIC_RELAXED, __HIP_MEMORY_SCOPE_AGENT));
      if (v >= 16u) break;
      __builtin_amdgcn_s_sleep(1);
    }
    PW_STAMP;
  } else idle();
  raw_bar();
}

constexpr double PI_ = 3.14159265358979323846;
constexpr double ccos_(double x) {
  double t = 1, s = 1;
  for (int k = 1; k <= 16; ++k) { t *= -x * x / ((2 * k - 1) * (2 * k)); s += t; }
  return s;
}
constexpr double c2048(int k) { k &= 2047; k = k > 1024 ? 2048 - k : k; return ccos_(2 * PI_ * k / 2048); }
constexpr u32 fb(double x) { return __builtin_bit_cast(u32, (float)x); }

struct ColTask { int a1, b1, am, bm; bool valid; };
constexpr __host__ __device__ ColTask col_task(int g, int p) {
  if (g < 150) { const int a1 = g / 10 + 1, b1 = 2 * (g % 10) + p; return {a1, b1, 32 - a1, 19 - b1, true}; }
  if (g < 155) { const int b1 = 2 * (g - 150) + p; return {16, b1, 16, 19 - b1, true}; }
  if (g == 155) return {0, 0, 0, 0, p == 0};
  if (g == 156) return {0, 10, 0, 10, p == 0};
  const int b1 = 2 * (g - 157) + 1 + p;
  return {0, b1 < 10 ? b1 : 9, 0, b1 < 10 ? 20 - b1 : 11, b1 < 10};
}
struct SlotMap {
  u32 v[768];
  constexpr SlotMap() : v() {
    for (int slot = 0; slot < NSLOT; ++slot) {
      const int mir = (slot >> 1) & 1;
      const ColTask t = col_task(slot >> 2, slot & 1);
      const int q1 = t.a1 + 32 * t.b1, a = mir ? t.am : t.a1, b = mir ? t.bm : t.b1;
      const bool cj = mir ? !(q1 > 320) : (q1 > 320);
      v[slot] = (u32)(33 * b + a) | (cj ? 0x80000000u : 0u);
    }
  }
};
template <int SD> struct ThetaTab {
  u32 v[4 * 256];
  constexpr ThetaTab() : v() {
    for (int l = 0; l < 64; ++l)
      for (int n = 0; n < 8; ++n) {
        const int p = 16 * SD + (l & 15) + 32 * crow(4 * (n >> 1) + 2 * ((l >> 4) & 1) + (n & 1), l >> 5);
        v[((n >> 2) * 64 + l) * 4 + (n & 3)] = fb(c2048(p));
        v[((2 + (n >> 2)) * 64 + l) * 4 + (n & 3)] = fb(c2048(p + 1536));
      }
  }
};
struct Theta { ThetaTab<0> a; ThetaTab<1> b; };
static __device__ const __attribute__((aligned(128))) SlotMap g_smap{};
static __device__ const __attribute__((aligned(128))) Theta g_theta{};

DI void dft32_consts(float (&c)[16], float (&s)[16], int r, int h) {
  const float rf = (float)r * (1.f / 32.f), r4 = rf * (float)(4 * h);
#pragma unroll
  for (int i = 0; i < 16; ++i) { const float rev = rf * (float)crow(i, 0) + r4; c[i] = cosr(rev); s[i] = sinr(rev); }
}
DI void pack_csn(const float (&c)[16], const float (&s)[16], u32x4 (&Cp)[2], u32x4 (&Sp)[2], u32x4 (&Np)[2]) {
#pragma unroll
  for (int i = 0; i < 8; ++i) {
    Cp[i >> 2][i & 3] = pk_f16(c[2 * i], c[2 * i + 1]);
    Sp[i >> 2][i & 3] = pk_f16(s[2 * i], s[2 * i + 1]);
    Np[i >> 2][i & 3] = pk_f16(-s[2 * i], -s[2 * i + 1]);
  }
}

constexpr int K1_IMG = 1344;
constexpr int K1_PLANE = 33 * 20 * 4;
constexpr int K1_PL0 = 8 * K1_IMG;
constexpr int K1_LDS = 22528;
constexpr int K1_WT_WGS = 128;
constexpr int K1_GRID = 256 + K1_WT_WGS;
static_assert(K1_PL0 + 4 * K1_PLANE <= K1_LDS, "LDS");

DI void k1_row(const u16* __restrict__ x, char* __restrict__ X1, char* smem, int wg) {
  const int tid = threadIdx.x, lane = tid & 63, w = wave_id(), r = lane & 31, h = lane >> 5;
  const int b2 = wg >> 3, aq = wg & 7;
  const int m = 32 * (4 * aq + w) + b2;
  u32x2 v[5];
  u32 e[3];
  {
    const char* src = (const char*)x + (size_t)m * 2560;
    const u32 vo = h * 1280 + r * 8;
    asm volatile("global_load_dwordx2 %0, %5, %6\n\tglobal_load_dwordx2 %1, %5, %6 offset:256\n\t"
                 "global_load_dwordx2 %2, %5, %6 offset:512\n\tglobal_load_dwordx2 %3, %5, %6 offset:768\n\t"
                 "global_load_dwordx2 %4, %5, %6 offset:1024"
                 : "=&v"(v[0]), "=&v"(v[1]), "=&v"(v[2]), "=&v"(v[3]), "=&v"(v[4]) : "v"(vo), "s"(src) : "memory");
    asm volatile("global_load_dword %0, %3, %4\n\tglobal_load_dword %1, %3, %4 offset:1024\n\t"
                 "global_load_dword %2, %3, %4 offset:2048"
                 : "=&v"(e[0]), "=&v"(e[1]), "=&v"(e[2]) : "v"(tid * 4), "s"(g_smap.v) : "memory");
  }
  u32x4 C1[2], S1[2], N1[2], C2, S2, N2, MR, MI;
  f32x2 tc[6], ts[6];
  {
    float c[16], s[16];
    dft32_consts(c, s, r, h);
    pack_csn(c, s, C1, S1, N1);
    const float rf = (float)r * (1.f / 640.f), r4 = rf * (float)(4 * h);
#pragma unroll
    for (int i = 0; i < 12; ++i) {
      const float rev = rf * (float)crow(i, 0) + r4;
      tc[i >> 1][i & 1] = cosr(rev); ts[i >> 1][i & 1] = sinr(rev);
      const float rev2 = rev * 32.f;
      c[i] = cosr(rev2); s[i] = sinr(rev2);
    }
#pragma unroll
    for (int i = 0; i < 4; ++i) {
      C2[i] = pk_f16(c[2 * i], c[2 * i + 1]); S2[i] = pk_f16(s[2 * i], s[2 * i + 1]); N2[i] = pk_f16(-s[2 * i], -s[2 * i + 1]);
    }
#pragma unroll
    for (int i = 0; i < 2; ++i) {
      const u32 cc = h ? 0u : pk_f16(c[8 + 2 * i], c[9 + 2 * i]), ss = h ? 0u : pk_f16(s[8 + 2 * i], s[9 + 2 * i]);
      MR[i] = cc; MR[2 + i] = ss; MI[i] = ss ^ 0x80008000u; MI[2 + i] = cc;
    }
#pragma unroll
    for (int i = 0; i < 2; ++i) { pin(C1[i]); pin(S1[i]); pin(N1[i]); }
    pin(C2); pin(S2); pin(N2); pin(MR); pin(MI);
#pragma unroll
    for (int i = 0; i < 6; ++i) { pin(tc[i]); pin(ts[i]); }
  }
  STAMP(X1 - X1_OFF, 1, 5);
  asm volatile("s_waitcnt vmcnt(3)" : "+v"(v[0]), "+v"(v[1]), "+v"(v[2]), "+v"(v[3]), "+v"(v[4])::"memory");
  STAMP(X1 - X1_OFF, 1, 1);
  f32x2 f[10];
#pragma unroll
  for (int i = 0; i < 5; ++i) {
    f[2 * i] = f32x2{bf_lo(v[i][0]), bf_hi(v[i][0])};
    f[2 * i + 1] = f32x2{bf_lo(v[i][1]), bf_hi(v[i][1])};
  }
  f32x2 s2 = f[0];
#pragma unroll
  for (int i = 1; i < 10; ++i) s2 += f[i];
  const float mean = sum32(s2[0] + s2[1]) * (1.f / 640.f);
  f32x2 q2 = {0.f, 0.f};
#pragma unroll
  for (int i = 0; i < 10; ++i) { const f32x2 d = f[i] - mean; q2 += d * d; }
  const float var = sum32(q2[0] + q2[1]) * (1.f / 640.f);
  float mb, inv;
  ln_scalars(mean, var, mb, inv);
  char* img = smem + w * (2 * K1_IMG);
#pragma unroll
  for (int i = 0; i < 5; ++i) {
    u32x2 o;
#pragma unroll
    for (int k = 0; k < 2; ++k) {
      const f32x2 d0 = f[2 * i + k] - mb;
      const u32 d = pk_bf16(d0[0], d0[1]);
      const f32x2 n0 = f32x2{bf_lo(d), bf_hi(d)} * inv;
      const u32 n = pk_bf16(n0[0], n0[1]);
      o[k] = pk_f16(bf_lo(n), bf_hi(n));
    }
    *(u32x2*)(img + h * K1_IMG + 8 * (r + 32 * i)) = o;
  }
  if (r < 8) { u32x2 z = {0u, 0u}; *(u32x2*)(img + h * K1_IMG + 1280 + 8 * r) = z; }
  asm volatile("" ::: "memory");
  STAMP(X1 - X1_OFF, 1, 2);
  const int q4 = (lane & 15) >> 2, p4 = lane & 3, blk = (lane >> 4) & 1;
  const char* tb = img + (4 * h + q4) * 40 + 32 * blk + 8 * p4;
  f32x16 Dr = zero16(), Di = zero16();
#pragma unroll
  for (int s = 0; s < 2; ++s) {
    u32x2 r0 = tr_read(tb + 640 * s), r1 = tr_read(tb + 640 * s + 320);
    u32x2 i0 = tr_read(tb + K1_IMG + 640 * s), i1 = tr_read(tb + K1_IMG + 640 * s + 320);
    u32x4 ar = {r0[0], r0[1], r1[0], r1[1]}, ai = {i0[0], i0[1], i1[0], i1[1]};
    Dr = mf16(ar, C1[s], Dr); Di = mf16(ai, C1[s], Di);
    Dr = mf16(ai, S1[s], Dr); Di = mf16(ar, N1[s], Di);
  }
  u32 er[6], ei[6];
#pragma unroll
  for (int k = 0; k < 6; ++k) {
    const f32x2 dr = lo2(Dr, 2 * k), di = lo2(Di, 2 * k);
    er[k] = pk_f16v(dr * tc[k] + di * ts[k]);
    ei[k] = pk_f16v(di * tc[k] - dr * ts[k]);
  }
  const u32x4 pr = {er[0], er[1], er[2], er[3]}, pi = {ei[0], ei[1], ei[2], ei[3]}, pm = {er[4], er[5], ei[4], ei[5]};
  f32x16 Xr = zero16(), Xi = zero16();
  Xr = mf16(C2, pr, Xr); Xi = mf16(N2, pr, Xi);
  Xr = mf16(S2, pi, Xr); Xi = mf16(C2, pi, Xi);
  Xr = mf16(MR, pm, Xr); Xi = mf16(MI, pm, Xi);
  char* planes = smem + K1_PL0;
  u32* pl = (u32*)(planes + w * K1_PLANE) + 33 * 4 * h + r;
#pragma unroll
  for (int i = 0; i < 8; ++i) pl[33 * crow(i, 0)] = pk_f16(Xr[i], Xi[i]);
  if (h == 0) {
#pragma unroll
    for (int i = 8; i < 12; ++i) pl[33 * crow(i, 0)] = pk_f16(Xr[i], Xi[i]);
  }
  STAMP(X1 - X1_OFF, 1, 3);
  asm volatile("s_waitcnt vmcnt(0) lgkmcnt(0)" : "+v"(e[0]), "+v"(e[1]), "+v"(e[2])::"memory");
  raw_bar();
  STAMP(X1 - X1_OFF, 1, 4);
  u32x4* dst = (u32x4*)(X1 + (size_t)(wg >> 1) * (NSLOT * 32)) + (aq & 1) * 4 + (tid >> 2) * 8 + (tid & 3);
#pragma unroll
  for (int it = 0; it < 3; ++it) {
    if (it < 2 || tid < NSLOT - 512) {
      const u32* sp = (const u32*)planes + (e[it] & 0xffffu);
      const u32 cj = e[it] & 0x80000000u;
      u32x4 o = {sp[0] ^ cj, sp[K1_PLANE / 4] ^ cj, sp[2 * (K1_PLANE / 4)] ^ cj, sp[3 * (K1_PLANE / 4)] ^ cj};
      dst[512 * it] = o;
    }
  }
}

DI int wt_off(int i, int h, int a) { return (((a >> 4) * 4 + (i >> 1)) * 64 + h * 32 + (i & 1) * 16 + (a & 15)) * 16; }

DI void k1_wt(const u16* __restrict__ wr, const u16* __restrict__ wi, char* __restrict__ WT, char* smem, int wg) {
  const int tid = threadIdx.x, lane = tid & 63, w = wave_id();
  const int qh = wg & 1, ao = (wg >> 1) & 3, h = (wg >> 3) & 1, i = wg >> 4;
  const int p0 = 8 * ao + 32 * (crow(2 * i, 0) + 4 * h), q0 = 161 * qh, nq = 161 - qh;
  {
    const char* src = (const char*)(w >> 1 ? wi : wr);
    const u32 lds = lds_addr(smem) + (w >> 1) * 11264;
#pragma unroll
    for (int n = 0; n < 6; ++n) {
      const int ins = (w & 1) * 6 + n;
      if (ins < 11) {
        const int e = ins * 64 + lane, row = e / 22, c = e - 22 * row;
        const int p = p0 + (row & 7) + ((row >> 3) & 1) * 1024 + (row >> 4) * 32;
        const u32 vo = min((u32)((642 * p + 2 * q0) & ~15) + 16u * c, 2048u * 642u - 16u);
        dma<1>(vo, src, lds + ins * 1024);
      }
    }
  }
  wait_vm<0>();
  raw_bar();
  const int alo = tid & 7, a = 8 * ao + alo;
  const char* lb = smem + alo * 352 + 2 * ((alo + q0) & 7) + 2 * (tid >> 3);
  char* ob = WT + (size_t)(q0 + (tid >> 3)) * 8192 + wt_off(i, h, a);
#pragma unroll
  for (int it = 0; it < 6; ++it) {
    const int ql = (tid >> 3) + 32 * it;
    if (ql < nq) {
      u32x4 o;
#pragma unroll
      for (int d = 0; d < 4; ++d)
        o[d] = (u32) * (const u16*)(lb + 64 * it + d * 2816) | ((u32) * (const u16*)(lb + 64 * it + d * 2816 + 11264) << 16);
      *(u32x4*)(ob + (size_t)it * (32 * 8192)) = o;
      if (it == 0 || it == 4) {
        const int q = q0 + ql;
        if (q == 0 || q == 320) {
          char* mb = WT + (size_t)(q ? 322 : 321) * 8192;
#pragma unroll
          for (int d = 0; d < 4; ++d) {
            const int k = p0 + alo + (d & 1) * 1024 + (d >> 1) * 32, kp = (2048 - k) & 2047;
            const int pp = kp & 1023, half = kp >> 10, a2 = pp & 31, bb = pp >> 5;
            const int e = (bb & 3) + 4 * (bb >> 3);
            *(u32*)(mb + wt_off(e >> 1, (bb >> 2) & 1, a2) + (e & 1) * 8 + half * 4) = o[d];
          }
        }
      }
    }
  }
}

__global__ __launch_bounds__(256, 2) void k1_kernel(const u16* __restrict__ x, const u16* __restrict__ wr,
                                                    const u16* __restrict__ wi, char* __restrict__ ws) {
  __shared__ __attribute__((aligned(16))) char smem[K1_LDS];
  const int bid = blockIdx.x;
  STAMP(ws, 1, 0);
  if (bid < 256) k1_row(x, ws + X1_OFF, smem, (bid & 7) * 32 + (bid >> 3));
  else {
    if (bid == 256 && threadIdx.x < 48) *(u32*)(ws + CNT_OFF + threadIdx.x * 256) = 0u;
    k1_wt(wr, wi, ws + WT_OFF, smem, bid - 256);
  }
  STAMP(ws, 1, 13);
  STAMPE(ws, 1);
}

constexpr int K2_X = 0, K2_ST = 0, K2_W = 16384, K2_TH = 32768, K2_E1 = 40960, K2_E2 = 49152, K2_EB = 57344;
constexpr int K2_LDS = 80 * 1024;
constexpr int K2_GRID = 256;
static_assert(K2_EB + 64 <= K2_LDS, "LDS");

DI void k2_col(const char* __restrict__ X1, const char* __restrict__ WT, char* __restrict__ X2, unsigned char* __restrict__ ecol,
               char* smem, int wg) {
  const int tid = threadIdx.x, lane = tid & 63, w = wave_id(), r = lane & 31, h = lane >> 5;
  const int pw = w & 1, sd = w >> 1, rho = (lane >> 4) & 1;
  const u32 lds0 = lds_addr(smem);
  const u32 l16 = lane * 16;
  {
    const char* xb = X1 + (size_t)wg * 128 + (size_t)w * (NSLOT * 32);
#pragma unroll
    for (int n = 0; n < 4; ++n) {
      const int rr = n * 8 + (lane >> 3);
      const u32 vo = (u32)rr * (u32)(4 * NSLOT * 32) + (u32)(((lane & 7) ^ ((rr >> 1) & 7)) * 16);
      dma<1>(vo, xb, lds0 + K2_X + (w * 4 + n) * 1024);
    }
  }
  const ColTask tk = col_task(wg, pw);
  const int a1 = tk.a1, b1 = tk.b1, am = tk.am, bm = tk.bm;
  const bool valid = tk.valid;
  const bool direct = wg >= 155, self = wg == 155 || wg == 156;
  const int q1 = a1 + 32 * b1, qm = am + 32 * bm;
  const bool swp = q1 > 320;
  const int task = (self && pw) ? (wg == 155 ? 321 : 322) : (swp ? qm : q1);
  dma<4>(l16, WT + (size_t)task * 8192 + sd * 4096, lds0 + K2_W + (pw * 2 + sd) * 4096);
  dma<2>(l16, (const char*)&g_theta + w * 2048, lds0 + K2_TH + w * 2048);
  u32x4 BR[4], BI[4], Cp[2], Sp[2], Np[2];
  f32x2 tc[8], ts[8];
  {
    float c[16], s[16];
    dft32_consts(c, s, r, h);
    pack_csn(c, s, Cp, Sp, Np);
#pragma unroll
    for (int i = 0; i < 16; ++i) { BR[i >> 2][i & 3] = pk_f16(c[i], s[i]); BI[i >> 2][i & 3] = pk_f16(-s[i], c[i]); }
    const float rf = (float)r * (1.f / 1024.f), r4 = rf * (float)(4 * h);
#pragma unroll
    for (int i = 0; i < 16; ++i) {
      const float rev = rf * (float)crow(i, 0) + r4;
      tc[i >> 1][i & 1] = cosr(rev); ts[i >> 1][i & 1] = sinr(rev);
    }
#pragma unroll
    for (int i = 0; i < 4; ++i) { pin(BR[i]); pin(BI[i]); }
#pragma unroll
    for (int i = 0; i < 2; ++i) { pin(Cp[i]); pin(Sp[i]); pin(Np[i]); }
#pragma unroll
    for (int i = 0; i < 8; ++i) { pin(tc[i]); pin(ts[i]); }
  }
  STAMPN(X2 - X2_OFF, 2, 9);
  wait_vm<6>();
  raw_bar();
  STAMPF(X2 - X2_OFF, 2, 9);
  STAMP(X2 - X2_OFF, 2, 1);
  const int slot = (((sd == 1) != swp) ? 2 : 0) + pw;
  f32x16 Zr = zero16(), Zi = zero16();
  {
    const char* xr = smem + K2_X + r * 128 + (((h * 4 + slot) ^ ((r >> 1) & 7)) * 16);
#pragma unroll
    for (int ks = 0; ks < 4; ++ks) {
      const u32x4 a = *(const u32x4*)(xr + ks * 4096);
      Zr = mf16(a, BR[ks], Zr); Zi = mf16(a, BI[ks], Zi);
    }
  }
  STAMP(X2 - X2_OFF, 2, 2);
  u32x4 P[4];
#pragma unroll
  for (int k = 0; k < 8; ++k) {
    const f32x2 dr = lo2(Zr, 2 * k), di = lo2(Zi, 2 * k);
    P[k >> 2][k & 3] = pk_f16v(dr * tc[k] + di * ts[k]);
    P[2 + (k >> 2)][k & 3] = pk_f16v(di * tc[k] - dr * ts[k]);
  }
  {
    u32x4* e1w = (u32x4*)(smem + K2_E1 + w * 2048) + (h * 16 + (lane & 15));
    const u32x4* e1r = (const u32x4*)(smem + K2_E1 + (w ^ 2) * 2048) + (h * 16 + (lane & 15));
    if (rho != sd) {
#pragma unroll
      for (int f = 0; f < 4; ++f) e1w[32 * f] = P[f];
    }
    wait_vm<0>();
    wait_lgkm0();
    raw_bar();
    if (rho != sd) {
#pragma unroll
      for (int f = 0; f < 4; ++f) P[f] = e1r[32 * f];
    }
  }
  STAMP(X2 - X2_OFF, 2, 3);
  Zr = zero16(); Zi = zero16();
#pragma unroll
  for (int s = 0; s < 2; ++s) {
    Zr = mf16(Cp[s], P[s], Zr); Zi = mf16(Cp[s], P[2 + s], Zi);
    Zr = mf16(Sp[s], P[2 + s], Zr); Zi = mf16(Np[s], P[s], Zi);
  }
  f32x2 alr[4], ali[4], ber[4], bei[4], dcr[4], dci[4];
  {
    const u32x4* wl = (const u32x4*)(smem + K2_W + (pw * 2 + sd) * 4096) + lane;
    const u32x4* wsl = (const u32x4*)(smem + K2_W + (2 + sd) * 4096) + lane;
    const f32x4* th = (const f32x4*)(smem + K2_TH + sd * 4096) + lane;
#pragma unroll
    for (int g = 0; g < 4; ++g) {
      const u32x4 t = wl[64 * g];
      f32x2 w0r = {bf_lo(t[0]), bf_lo(t[2])}, w0i = {bf_hi(t[0]), bf_hi(t[2])};
      f32x2 w1r = {bf_lo(t[1]), bf_lo(t[3])}, w1i = {bf_hi(t[1]), bf_hi(t[3])};
      if (self) {
        const u32x4 u = wsl[64 * g];
        w0r = (w0r + f32x2{bf_lo(u[0]), bf_lo(u[2])}) * 0.5f; w0i = (w0i - f32x2{bf_hi(u[0]), bf_hi(u[2])}) * 0.5f;
        w1r = (w1r + f32x2{bf_lo(u[1]), bf_lo(u[3])}) * 0.5f; w1i = (w1i - f32x2{bf_hi(u[1]), bf_hi(u[3])}) * 0.5f;
      }
      const f32x4 thc = th[64 * (g >> 1)], ths = th[64 * (2 + (g >> 1))];
      const f32x2 ct = pr2(thc, 2 * (g & 1)), st = pr2(ths, 2 * (g & 1));
      const f32x2 sr = w0r + w1r, si = w0i + w1i, dr = w0r - w1r, di = w0i - w1i;
      alr[g] = sr - dr * st; ali[g] = si - di * st; ber[g] = sr + dr * st; bei[g] = si + di * st;
      dcr[g] = dr * ct; dci[g] = di * ct;
    }
  }
  STAMP(X2 - X2_OFF, 2, 4);
  f32x2 nur[4], nui[4], nvr[4], nvi[4];
  float mx = 0.f;
#pragma unroll
  for (int g = 0; g < 4; ++g) {
    f32x2 ur, ui, vr, vi;
#pragma unroll
    for (int k = 0; k < 2; ++k) {
      auto sr_ = __builtin_amdgcn_permlane16_swap(asu(Zr[4 * g + k]), asu(Zr[4 * g + 2 + k]), false, false);
      auto si_ = __builtin_amdgcn_permlane16_swap(asu(Zi[4 * g + k]), asu(Zi[4 * g + 2 + k]), false, false);
      ur[k] = asf(sr_[0]); vr[k] = asf(sr_[1]); ui[k] = asf(si_[0]); vi[k] = asf(si_[1]);
    }
    nur[g] = alr[g] * ur - ali[g] * ui - dcr[g] * vi - dci[g] * vr;
    nui[g] = alr[g] * ui + ali[g] * ur + dcr[g] * vr - dci[g] * vi;
    nvr[g] = ber[g] * vr - bei[g] * vi + dcr[g] * ui + dci[g] * ur;
    nvi[g] = ber[g] * vi + bei[g] * vr - dcr[g] * ur + dci[g] * ui;
#pragma unroll
    for (int k = 0; k < 2; ++k)
      mx = fmaxf(fmaxf(mx, fmaxf(fabsf(nur[g][k]), fabsf(nui[g][k]))), fmaxf(fabsf(nvr[g][k]), fabsf(nvi[g][k])));
  }
  const int eb = (int)umax(max64(asu(mx)) >> 23, 4u);
  const float sc = asf((u32)(258 - eb) << 23);
#pragma unroll
  for (int g = 0; g < 4; ++g) {
    auto qr = __builtin_amdgcn_permlane16_swap(pk_f16v(nur[g] * sc), pk_f16v(nvr[g] * sc), false, false);
    auto qi = __builtin_amdgcn_permlane16_swap(pk_f16v(nui[g] * sc), pk_f16v(nvi[g] * sc), false, false);
    P[g >> 1][2 * (g & 1)] = qr[0]; P[g >> 1][2 * (g & 1) + 1] = qr[1];
    P[2 + (g >> 1)][2 * (g & 1)] = qi[0]; P[2 + (g >> 1)][2 * (g & 1) + 1] = qi[1];
  }
  STAMP(X2 - X2_OFF, 2, 5);
  Zr = zero16(); Zi = zero16();
#pragma unroll
  for (int s = 0; s < 2; ++s) {
    Zr = mf16(P[s], Cp[s], Zr); Zi = mf16(P[s], Sp[s], Zi);
    Zr = mf16(P[2 + s], Np[s], Zr); Zi = mf16(P[2 + s], Cp[s], Zi);
  }
  int em;
  auto finish = [&](auto SD) {
    constexpr int S = decltype(SD)::v, KO = 8 * S, SO = 8 - KO;
    u32x4 kr, ki, orr, oi;
    u32x4* e2w = (u32x4*)(smem + K2_E2 + w * 2048) + lane;
    const u32x4* e2r = (const u32x4*)(smem + K2_E2 + (w ^ 2) * 2048) + lane;
    int* ebx = (int*)(smem + K2_EB);
    if (lane == 0) ebx[w] = eb;
#pragma unroll
    for (int k = 0; k < 4; ++k) {
      const f32x2 dr = lo2(Zr, SO + 2 * k), di = lo2(Zi, SO + 2 * k), c = tc[4 * S + k], sn = ts[4 * S + k];
      orr[k] = pk_f16v(dr * c - di * sn); oi[k] = pk_f16v(di * c + dr * sn);
    }
    e2w[0] = orr; e2w[64] = oi;
#pragma unroll
    for (int k = 0; k < 4; ++k) {
      const f32x2 dr = lo2(Zr, KO + 2 * k), di = lo2(Zi, KO + 2 * k), c = tc[4 * S + k], sn = ts[4 * S + k];
      kr[k] = pk_f16v(dr * c - di * sn); ki[k] = pk_f16v(di * c + dr * sn);
    }
    wait_lgkm0();
    raw_bar();
    orr = e2r[0]; oi = e2r[64];
    const int eo = __builtin_amdgcn_readfirstlane(ebx[w ^ 2]);
    const int ebu = __builtin_amdgcn_readfirstlane(eb);
    em = max(ebu, eo);
    if (ebu != eo) {
      const u32 fk = pow2_h2(ebu - em), fo = pow2_h2(eo - em);
#pragma unroll
      for (int i = 0; i < 4; ++i) { kr[i] = pkmul_h(kr[i], fk); ki[i] = pkmul_h(ki[i], fk); orr[i] = pkmul_h(orr[i], fo); oi[i] = pkmul_h(oi[i], fo); }
    }
    Zr = zero16(); Zi = zero16();
    Zr = mf16(Cp[S], kr, Zr); Zi = mf16(Sp[S], kr, Zi);
    Zr = mf16(Np[S], ki, Zr); Zi = mf16(Cp[S], ki, Zi);
    Zr = mf16(Cp[1 - S], orr, Zr); Zi = mf16(Sp[1 - S], orr, Zi);
    Zr = mf16(Np[1 - S], oi, Zr); Zi = mf16(Cp[1 - S], oi, Zi);
  };
  if (sd) finish(IC<1>{}); else finish(IC<0>{});
  const bool prim = (sd == 1) == swp;
  const int ao = prim ? a1 : am, bo = prim ? b1 : bm;
  const u32 cj = sd ? 0x80000000u : 0u;
  if (valid && lane == 0) ecol[ao * 20 + bo] = (unsigned char)em;
  STAMP(X2 - X2_OFF, 2, 7);
  if (!direct) {
    u32x4* su = (u32x4*)(smem + K2_ST) + (((prim ? 0 : 2) + (bo & 1)) * 4) * 64 + lane;
#pragma unroll
    for (int g = 0; g < 4; ++g) {
      u32x4 o = {pk_f16(Zr[4 * g], Zi[4 * g]) ^ cj, pk_f16(Zr[4 * g + 1], Zi[4 * g + 1]) ^ cj,
                 pk_f16(Zr[4 * g + 2], Zi[4 * g + 2]) ^ cj, pk_f16(Zr[4 * g + 3], Zi[4 * g + 3]) ^ cj};
      su[64 * g] = o;
    }
    wait_lgkm0();
    raw_bar();
    STAMP(X2 - X2_OFF, 2, 8);
    const int c2 = b1 >> 1;
#pragma unroll
    for (int set = 0; set < 2; ++set) {
      const int duo = set ? (am * 10 + 9 - c2) : (a1 * 10 + c2);
      const u32x4* sp = (const u32x4*)(smem + K2_ST) + (set * 8 + w) * 64 + lane;
      const u32x4 p0 = sp[0], p1 = sp[256];
      u32x2* dp = (u32x2*)(X2 + (size_t)(duo >> 1) * 16384) + (duo & 1) + 2 * (32 * (8 * w + 4 * h) + r);
#pragma unroll
      for (int i = 0; i < 4; ++i) { u32x2 o = {p0[i], p1[i]}; dp[64 * i] = o; }
    }
  } else if (valid && !(self && sd)) {
    u32* du = (u32*)(X2 + (size_t)(bo >> 2) * 16384) + (bo & 3) + 4 * (128 * h + r);
#pragma unroll
    for (int i = 0; i < 16; ++i) du[128 * crow(i, 0)] = pk_f16(Zr[i], Zi[i]) ^ cj;
  }
}

constexpr int K2_W1_MAT = 19 * 1024 + 128;
DI void k2_w1(const u16* __restrict__ wg_, const u16* __restrict__ wu_, char* __restrict__ W1, char* smem, int idx) {
  const int lane = threadIdx.x & 63, w = wave_id();
  const int t = idx >> 2, mat = w >> 1;
  const char* src = (const char*)(mat ? wu_ : wg_);
  const u32 lds0 = lds_addr(smem);
#pragma unroll
  for (int un = 0; un < 2; ++un) {
    const int u = 2 * (idx & 3) + un;
#pragma unroll
    for (int n = 0; n < 10; ++n) {
      const int ins = min((w & 1) * 10 + n, 18);
      const int e = min(ins * 64 + lane, 1199), row = e / 15, c = e - 15 * row;
      const u32 vo = min((u32)((80 * u + row) * 3456 + (216 * t & ~15) + 16 * c), 640u * 3456u - 16u);
      dma<1>(vo, src, lds0 + (un * 2 + mat) * K2_W1_MAT + ins * 1024);
    }
  }
  const int h2_ = lane >> 5, blk = (lane >> 4) & 1, q4 = (lane & 15) >> 2, p4 = lane & 3;
#pragma unroll
  for (int un = 0; un < 2; ++un) {
    const int u = 2 * (idx & 3) + un;
    if (un == 0) wait_vm<10>(); else wait_vm<9>();
    raw_bar();
    const char* lb = smem + (un * 2 + blk) * K2_W1_MAT + (t & 1) * 8 + 8 * p4;
    u32x4 o[9];
#pragma unroll
    for (int i = 0; i < 9; ++i) {
      const int fr = min(w + 4 * i, 34), kk = fr / 7, ft = fr - 7 * kk;
      const int r0 = kk < 4 ? 20 * kk + 4 * h2_ + q4 : 40 * h2_ + 16 + q4;
      const int r1 = kk < 4 ? r0 + 8 : r0 + 20;
      const u32x2 lo = tr_read(lb + r0 * 240 + 32 * ft), hi = tr_read(lb + r1 * 240 + 32 * ft);
      o[i] = u32x4{lo[0], lo[1], hi[0], hi[1]};
    }
#pragma unroll
    for (int i = 0; i < 9; ++i) {
      const int fr = min(w + 4 * i, 34), kk = fr / 7, ft = fr - 7 * kk;
      const int k16 = kk < 4 ? 4 * u + kk : 32 + u;
      const int pos = ft < 3 ? k16 * 3 + ft : 40 * ft + k16;
      if (ft == 6 && (lane & 15) >= 12) o[i] = u32x4{0u, 0u, 0u, 0u};
      *((u32x4*)(W1 + ((size_t)t * 280 + pos) * 1024) + lane) = o[i];
    }
  }
}
DI void k2_w2(const u16* __restrict__ wd, char* __restrict__ W2, char* smem, int idx) {
  const int lane = threadIdx.x & 63, w = wave_id();
  const u32 lds0 = lds_addr(smem);
#pragma unroll
  for (int un = 0; un < 2; ++un) {
    const int k32 = 2 * idx + un;
#pragma unroll
    for (int n = 0; n < 10; ++n) {
      const int e = (w * 10 + n) * 64 + lane, row = e / 80, c = e - 80 * row, e2 = row >> 4;
      const int t = k32 < 32 ? k32 >> 1 : (k32 < 48 ? k32 - 32 : 2 * (k32 - 48) + e2);
      const int ft = k32 < 32 ? 2 * (k32 & 1) + e2 : (k32 < 48 ? 4 + e2 : 6), fl = min(16 * ft + (row & 15), 107);
      dma<1>((u32)((108 * t + fl) * 1280 + ((c ^ ((row & 7) << 1)) * 16)), wd, lds0 + un * 40960 + (w * 10 + n) * 1024);
    }
  }
  const int g = lane >> 4, q4 = (lane & 15) >> 2, p4 = lane & 3;
  const int row = 16 * (g >> 1) + 4 * (g & 1) + q4, sw = (row & 7) << 1;
#pragma unroll
  for (int un = 0; un < 2; ++un) {
    const int k32 = 2 * idx + un;
    wait_vm<10>();
    raw_bar();
    const char* lb = smem + un * 40960 + row * 1280 + (p4 & 1) * 8;
#pragma unroll
    for (int i = 0; i < 10; ++i) {
      const int fr = w * 10 + i;
      const char* a = lb + (((2 * fr + (p4 >> 1)) ^ sw) * 16);
      u32x2 lo = tr_read(a), hi = tr_read(a + 8 * 1280);
      if (k32 >= 48 && (g & 1)) hi = u32x2{0u, 0u};
      const int nb = fr / 5, nt = fr - 5 * nb;
      *((u32x4*)(W2 + ((size_t)(nb * 56 + k32) * 5 + nt) * 1024) + lane) = u32x4{lo[0], lo[1], hi[0], hi[1]};
    }
  }
}
static_assert(4 * K2_W1_MAT <= K2_LDS && 81920 <= K2_LDS, "LDS");

__global__ __launch_bounds__(256, 2) void k2_kernel(const u16* __restrict__ wg_, const u16* __restrict__ wu_,
                                                    const u16* __restrict__ wd, char* __restrict__ ws) {
  __shared__ __attribute__((aligned(16))) char smem[K2_LDS];
  const int bid = blockIdx.x;
  STAMP(ws, 2, 0);
  const int lw = bid < 168 ? (bid & 7) * 21 + (bid >> 3) : bid;
  if (lw < NCOLWG) k2_col(ws + X1_OFF, ws + WT_OFF, ws + X2_OFF, (unsigned char*)(ws + EC_OFF), smem, lw);
  else if (lw < NCOLWG + 64) k2_w1(wg_, wu_, ws + W1_OFF, smem, lw - NCOLWG);
  else if (lw < NCOLWG + 92) k2_w2(wd, ws + W2_OFF, smem, lw - NCOLWG - 64);
  STAMP(ws, 2, 13);
  STAMPE(ws, 2);
}

constexpr int R_BASE = 120 * 1024;
constexpr int R_X2 = R_BASE, R_EC = R_BASE + 10240, R_ST = R_BASE + 12288, R_H = R_ST + 80 * 144;
static_assert(R_H + 10240 <= 160 * 1024, "LDS");
constexpr int PDN = 10, NWF = 12;
constexpr int bnd_slot(int c) { return 40 * c - PDN; }
constexpr int NEPI = 192;
constexpr int epi_slot(int u) { return u < 128 ? 121 + (u * 94) / 128 : 216 + ((u - 128) * 63) / 64; }
constexpr int epi_first(int n) { int u = 0; while (u < NEPI && epi_slot(u) < n) ++u; return u; }
constexpr int gst_slot(int e) { return epi_slot(32 * e + 31) + 1; }
constexpr int SIG_A = 248, POLL_A = 270;
constexpr int SIG_B = 280 + 5 * 2, POLL_B = 280 + 5 * 15, CHECK_B = 280 + 5 * 20;
struct Ev { int slot, kind, a, b; };
constexpr int ev_size(const Ev& e) { return e.kind == 0 ? 4 : e.kind == 1 ? (e.b == 0 ? 4 : 3) : e.kind == 5 ? 5 : e.kind == 4 ? 2 : 1; }
template <int N> struct Sched {
  Ev ev[N];
  int n;
  constexpr void add(int slot, int kind, int a, int b) {
    int i = n++;
    while (i > 0 && ev[i - 1].slot > slot) { ev[i] = ev[i - 1]; --i; }
    ev[i] = Ev{slot, kind, a, b};
  }
  constexpr int first(int slot) const { int i = 0; while (i < n && ev[i].slot < slot) ++i; return i; }
  constexpr int before(int slot) const { int v = 0; for (int i = 0; i < n; ++i) v += ev[i].slot < slot ? ev_size(ev[i]) : 0; return v; }
  constexpr int end_of(int kind, int a, int b) const {
    int v = 0;
    for (int i = 0; i < n; ++i) { v += ev_size(ev[i]); if (ev[i].kind == kind && ev[i].a == a && ev[i].b == b) return v; }
    return -1000;
  }
};
constexpr Sched<44> make_s4() {
  Sched<44> s{};
  for (int g = 0; g < 10; ++g) s.add(g < 3 ? -1 : 12 * (g - 3) + 1, 0, g, 0);
  const int c0[7] = {80, 100, 124, 151, 181, 205, 232}, dp[7] = {6, 8, 9, 9, 8, 7, 6};
  for (int c = 0; c < 7; ++c) for (int p = 0; p < 3; ++p) s.add(c0[c] + dp[c] * p, 1, 3 + c, p);
  for (int e = 0; e < 6; ++e) s.add(gst_slot(e), 3, e, 0);
  s.add(POLL_A, 2, 0, 0);
  return s;
}
constexpr Sched<76> make_s5() {
  Sched<76> s{};
  for (int j = 0; j < 28; ++j) s.add(j < 6 ? -1 : 280 + 10 * (j - 6) + 1, 4, j, 0);
  for (int c = 0; c < 4; ++c) for (int p = 0; p < 3; ++p) s.add(280 + 40 * c + 23 + 10 * p, 1, 10 + c, p);
  s.add(POLL_B, 2, 0, 0);
  s.add(280 + 225, 5, 0, 0);
  return s;
}
constexpr int cap63(int v) { return v < 63 ? v : 63; }
constexpr Sched<44> S4 = make_s4();
constexpr Sched<76> S5 = make_s5();

__global__ __launch_bounds__(256, 1) void k3_kernel(const u16* __restrict__ x, char* __restrict__ ws, u16* __restrict__ out) {
  __shared__ __attribute__((aligned(16))) char smem[160 * 1024];
  const int tid = threadIdx.x, lane = tid & 63, w = wave_id(), r = lane & 31, h = lane >> 5;
  const int bid = blockIdx.x, xcd = bid & 7, idx = bid >> 3;
  const int rb = 4 * (xcd & 3) + (idx & 3), t = 8 * (xcd >> 2) + (idx >> 2);
  const int mb = 2 * rb + (xcd >> 2), nb = idx >> 2;
  const u32 lds0 = lds_addr(smem), voff = lane * 16;
  const char* w1g = ws + W1_OFF + (size_t)t * (280 * 1024);
  const char* w2g = ws + W2_OFF + (size_t)nb * (280 * 1024);
  auto chunk_part = [&](auto C, auto P) {
    constexpr int c = decltype(C)::v, p = decltype(P)::v, o = p == 0 ? 0 : (p == 1 ? 4096 : 7168);
    const char* g = (c < 7 ? w1g + c * 40960 : w2g + (c - 7) * 40960) + w * 10240 + o;
    dma<(p == 0 ? 4 : 3)>(voff, g, lds0 + (c & 3) * 40960 + w * 10240 + o);
  };
  STAMP(ws, 3, 0);
  {
    const int wg = 16 * rb + t, m = 4 * wg + w;
    const char* X2 = ws + X2_OFF;
#pragma unroll
    for (int n = 0; n < 3; ++n) {
      const int pc = w + 4 * n;
      const u32 vx = (u32)(pc * 16 + (lane >> 2)) * 16384u + (u32)(wg * 64 + (lane & 3) * 16);
      const u32 ve = (u32)X2_SZ + (pc == 10 ? (u32)min(lane, 39) * 16u : 0u);
      dma<1>(pc < 10 ? vx : ve, X2, lds0 + R_X2 + pc * 1024);
    }
    u32x2 xr[6];
    {
      const char* src = (const char*)x + (size_t)m * 2560;
      const u32 vo = 40 * r + 8 * h, v2 = 40 * r + 32;
      asm volatile("global_load_dwordx2 %0, %6, %8\n\tglobal_load_dwordx2 %1, %6, %8 offset:16\n\tglobal_load_dwordx2 %2, %7, %8\n\t"
                   "global_load_dwordx2 %3, %6, %8 offset:1280\n\tglobal_load_dwordx2 %4, %6, %8 offset:1296\n\t"
                   "global_load_dwordx2 %5, %7, %8 offset:1280"
                   : "=&v"(xr[0]), "=&v"(xr[1]), "=&v"(xr[2]), "=&v"(xr[3]), "=&v"(xr[4]), "=&v"(xr[5])
                   : "v"(vo), "v"(v2), "s"(src) : "memory");
    }
    u32x4 BR[3], BI[3], Cp[2], Sp[2], Np[2];
    f32x2 tc[8], ts[8];
    {
      float c[16], s[16];
      dft32_consts(c, s, r, h);
      pack_csn(c, s, Cp, Sp, Np);
#pragma unroll
      for (int i = 0; i < 2; ++i) { pin(Cp[i]); pin(Sp[i]); pin(Np[i]); }
      const float rf = (float)r * (1.f / 640.f), r4 = rf * (float)(4 * h);
      static_for<0, 4>([&](auto Q) {
        constexpr int q = decltype(Q)::v;
#pragma unroll
        for (int i = 4 * q; i < 4 * q + 4; ++i) {
          const float rev = rf * (float)crow(i, 0) + r4;
          tc[i >> 1][i & 1] = cosr(rev); ts[i >> 1][i & 1] = sinr(rev);
          if (i < 12) {
            const float rev2 = rev * 32.f, c2 = cosr(rev2), s2 = sinr(rev2);
            const u32 br = pk_f16(c2, -s2), bi = pk_f16(s2, c2);
            BR[q][i & 3] = (i >= 8 && h) ? 0u : br; BI[q][i & 3] = (i >= 8 && h) ? 0u : bi;
          }
        }
        if constexpr (q < 3) { pin(BR[q]); pin(BI[q]); }
        pin(tc[2 * q]); pin(ts[2 * q]); pin(tc[2 * q + 1]); pin(ts[2 * q + 1]);
      });
    }
    wait_vm<6>();
    raw_bar();
    STAMP(ws, 3, 1);
    u32x4 a[3];
    u32 ec[3];
#pragma unroll
    for (int ks = 0; ks < 3; ++ks) {
      const int c = (ks == 2) ? 4 : 2 * ks + h;
      a[ks] = *(const u32x4*)(smem + R_X2 + (r * 5 + c) * 64 + w * 16);
      ec[ks] = *(const u32*)(smem + R_EC + (r * 5 + c) * 4);
    }
    int ex[12];
    u32 em = 0;
#pragma unroll
    for (int i = 0; i < 12; ++i) { ex[i] = (ec[i >> 2] >> (8 * (i & 3))) & 0xff; em = umax(em, (u32)ex[i]); }
    const int E = (int)max64(em);
#pragma unroll
    for (int i = 0; i < 12; ++i) a[i >> 2][i & 3] = pkmul_h(a[i >> 2][i & 3], pow2_h2(ex[i] - (E + 5)));
    const float fs = __builtin_amdgcn_ldexpf(1.f / 1280.f, E - 127 - 9);
    f32x16 Tr = zero16(), Ti = zero16();
#pragma unroll
    for (int ks = 0; ks < 3; ++ks) { Tr = mf16(a[ks], BR[ks], Tr); Ti = mf16(a[ks], BI[ks], Ti); }
    pin(Tr); pin(Ti);
    chunk_part(IC<0>{}, IC<0>{});
    u32x4 P[4];
#pragma unroll
    for (int k = 0; k < 8; ++k) {
      const f32x2 dr = lo2(Tr, 2 * k), di = lo2(Ti, 2 * k);
      P[k >> 2][k & 3] = pk_f16v(dr * tc[k] - di * ts[k]);
      P[2 + (k >> 2)][k & 3] = pk_f16v(di * tc[k] + dr * ts[k]);
    }
    pin(P[0]); pin(P[1]); pin(P[2]); pin(P[3]);
    chunk_part(IC<0>{}, IC<1>{});
    f32x16 Vr = zero16(), Vi = zero16();
#pragma unroll
    for (int s = 0; s < 2; ++s) {
      Vr = mf16(P[s], Cp[s], Vr); Vi = mf16(P[s], Sp[s], Vi);
      Vr = mf16(P[2 + s], Np[s], Vr); Vi = mf16(P[2 + s], Cp[s], Vi);
    }
    pin(Vr); pin(Vi);
    chunk_part(IC<0>{}, IC<2>{});
    asm volatile("s_waitcnt vmcnt(10)" : "+v"(xr[0]), "+v"(xr[1]), "+v"(xr[2]), "+v"(xr[3]), "+v"(xr[4]), "+v"(xr[5])::"memory");
    STAMP(ws, 3, 2);
    f32x2 hv[2][6];
    u32 hp[2][6];
    float mean[2], var[2];
#pragma unroll
    for (int rr = 0; rr < 2; ++rr) {
      f32x2 sm = {0.f, 0.f};
#pragma unroll
      for (int k = 0; k < 6; ++k) {
        const f32x2 y = (rr ? lo2(Vi, 2 * k) : lo2(Vr, 2 * k)) * fs;
        const u32 yb = pk_bf16(y[0], y[1]);
        const u32 xx = xr[3 * rr + (k >> 1)][k & 1];
        const f32x2 hs = f32x2{bf_lo(xx), bf_hi(xx)} + f32x2{bf_lo(yb), bf_hi(yb)};
        u32 hh = pk_bf16(hs[0], hs[1]);
        if (k >= 4) hh = h ? 0u : hh;
        hp[rr][k] = hh;
        hv[rr][k] = f32x2{bf_lo(hh), bf_hi(hh)};
        sm += hv[rr][k];
      }
      mean[rr] = sm[0] + sm[1];
    }
    pin(mean[0]); pin(mean[1]);
    chunk_part(IC<1>{}, IC<0>{});
#pragma unroll
    for (int rr = 0; rr < 2; ++rr) {
      char* hl = smem + R_H + (2 * w + rr) * 1280 + 40 * r;
      *(u32x2*)(hl + 8 * h) = u32x2{hp[rr][0], hp[rr][1]};
      *(u32x2*)(hl + 8 * h + 16) = u32x2{hp[rr][2], hp[rr][3]};
      if (h == 0) *(u32x2*)(hl + 32) = u32x2{hp[rr][4], hp[rr][5]};
    }
#pragma unroll
    for (int rr = 0; rr < 2; ++rr) mean[rr] = sum64(mean[rr]) * (1.f / 640.f);
    pin(mean[0]); pin(mean[1]);
    chunk_part(IC<1>{}, IC<1>{});
#pragma unroll
    for (int rr = 0; rr < 2; ++rr) {
      f32x2 q2 = {0.f, 0.f}, q3 = {0.f, 0.f};
#pragma unroll
      for (int k = 0; k < 4; ++k) { const f32x2 d = hv[rr][k] - mean[rr]; q2 += d * d; }
#pragma unroll
      for (int k = 4; k < 6; ++k) { const f32x2 d = hv[rr][k] - mean[rr]; q3 += d * d; }
      var[rr] = q2[0] + q2[1] + (h ? 0.f : q3[0] + q3[1]);
    }
    pin(var[0]); pin(var[1]);
    chunk_part(IC<1>{}, IC<2>{});
#pragma unroll
    for (int rr = 0; rr < 2; ++rr) var[rr] = sum64(var[rr]) * (1.f / 640.f);
#pragma unroll
    for (int rr = 0; rr < 2; ++rr) {
      float mbn, inv;
      ln_scalars(mean[rr], var[rr], mbn, inv);
      u32 o[6];
#pragma unroll
      for (int k = 0; k < 6; ++k) {
        const f32x2 d0 = hv[rr][k] - mbn;
        const u32 d = pk_bf16(d0[0], d0[1]);
        const f32x2 n0 = f32x2{bf_lo(d), bf_hi(d)} * inv;
        o[k] = pk_bf16(n0[0], n0[1]);
      }
      const int rowl = 2 * w + rr;
      u32x4 o4 = {o[0], o[1], o[2], o[3]};
      *(u32x4*)(smem + R_ST + (h * 32 + r) * 144 + rowl * 16) = o4;
      if (h == 0) { u32x2 o2 = {o[4], o[5]}; *(u32x2*)(smem + R_ST + (64 + (r >> 1)) * 144 + rowl * 16 + (r & 1) * 8) = o2; }
    }
    wait_lgkm0();
    raw_bar();
    STAMP(ws, 3, 3);
    const int row0 = 8 * wg;
    const char* ab = ws + ACT_OFF + (size_t)(row0 >> 5) * (40 * 1024) + (row0 & 31) * 16;
#pragma unroll
    for (int it = 0; it < 3; ++it) {
      const int gi = (tid >> 3) + 32 * it;
      if (gi < 80) {
        const int L = gi < 64 ? (gi & 1) * 32 + (gi >> 1) : gi;
        st16_dev(gi * 512 + (tid & 7) * 16, ab, *(const u32x4*)(smem + R_ST + L * 144 + (tid & 7) * 16));
      }
    }
  }
  STAMP(ws, 3, 4);
  auto pre1 = [&] {
    const char* hb = ws + H_OFF + (size_t)(16 * rb + t) * 10240;
#pragma unroll
    for (int i = 0; i < 4; ++i) {
      const int c = tid - 64 + 192 * i;
      if (c < 640) st16_dev(c * 16, hb, *(const u32x4*)(smem + R_H + c * 16));
    }
#pragma unroll
    for (int g = 0; g < 4; ++g)
      if (3 * g + w - 1 < 10) dma<4>(voff, w1g + 81920 + (3 * g + w - 1) * 4096, lds0 + 81920 + (3 * g + w - 1) * 4096);
  };
  publish_and_wait((u32*)(ws + CNT_OFF + rb * 256), tid, w, pre1);
  STAMP(ws, 3, 5);

  u32 lb[3];
#pragma unroll
  for (int i = 0; i < 3; ++i) { lb[i] = lane * 16 + i * 65536; pin(lb[i]); }
  u32x4 wf[NWF];
  u32 pollv;
  u32* cntA = (u32*)(ws + CNT_OFF + (16 + rb) * 256);
  u32* cntB = (u32*)(ws + CNT_OFF + (32 + rb) * 256);
  auto rdw = [&](auto N) {
    constexpr int n = decltype(N)::v, o = (n % 160) * 1024;
    wf[n % NWF] = *(const u32x4*)(smem + lb[o >> 16] + (o & 65535));
  };
  auto boundary = [&](auto WN) {
    wait_lgkm0();
    wait_vm<decltype(WN)::v>();
    raw_bar();
  };
  {
    u32x4 act[40];
    const char* actg = ws + ACT_OFF + (size_t)(4 * rb + w) * (40 * 1024);
    f32x16 acc[7];
    u32x4 gout;
    float ez[2][2];
    const int m = 128 * rb + 32 * w + r;
    const char* gdst = ws + G_OFF + (size_t)((2 * rb + (w >> 1)) * 56) * 4096 + (w & 1) * 2048;
    const u32 gvo = (r >> 4) * 1024 + (r & 15) * 16 + h * 256;
    float k1 = -2.3022081983f;
    pin(k1);
    auto epi = [&](auto U) {
      constexpr int u = decltype(U)::v, z = u & 1, st = (u >> 1) & 7, pr = 2 * (u >> 4) + z, ft = pr / 4, p = pr % 4;
#pragma unroll
      for (int e = 0; e < 2; ++e) {
        const float g = acc[ft][2 * p + e];
        if constexpr (st == 0) ez[z][e] = vmul(g, g);
        if constexpr (st == 1) ez[z][e] = vpoly(ez[z][e], k1);
        if constexpr (st == 2) ez[z][e] = vmul(ez[z][e], g);
        if constexpr (st == 3) ez[z][e] = vexp(ez[z][e]);
        if constexpr (st == 4) { ez[z][e] = vadd1(ez[z][e]); }
        if constexpr (st == 5) ez[z][e] = vrcp(ez[z][e]);
        if constexpr (st == 6) ez[z][e] = vmul(g, ez[z][e]);
        if constexpr (st == 7) ez[z][e] = vmul(ez[z][e], acc[ft][8 + 2 * p + e]);
      }
      if constexpr (st == 7) gout[p] = pk_bf16(ez[z][0], ez[z][1]);
    };
    auto gstore = [&](int ft) {
      const int kk = ft < 4 ? 2 * t + (ft >> 1) : (ft < 6 ? 32 + t : 48 + (t >> 1)), g2 = ft < 6 ? ft & 1 : t & 1;
      st16_dev(gvo, gdst + kk * 4096 + g2 * 512, gout);
    };
    auto events = [&](auto N) {
      static_for<S4.first(decltype(N)::v), S4.first(decltype(N)::v + 1)>([&](auto I) {
        constexpr Ev e = S4.ev[decltype(I)::v];
        {
          if constexpr (e.kind == 0) ld4_dev(act[4 * e.a], act[4 * e.a + 1], act[4 * e.a + 2], act[4 * e.a + 3], voff, actg + e.a * 4096);
          if constexpr (e.kind == 1) chunk_part(IC<e.a>{}, IC<e.b>{});
          if constexpr (e.kind == 3) gstore(e.a);
          if constexpr (e.kind == 2) poll_issue(pollv, cntA);
        }
      });
    };
    events(IC<-1>{});
    static_for<0, PDN>(rdw);
    static_for<0, 280>([&](auto N) {
      constexpr int n = decltype(N)::v;
      constexpr int ft = n < 120 ? n % 3 : n / 40, k16 = n < 120 ? n / 3 : n % 40;
      if constexpr (n < 120 && n % 6 == 0)
        wait_a2<S4.before(n) - (S4.end_of(0, k16 / 4, 0) - 2 + k16 % 4)>(act[k16], act[k16 + 1]);
      mfma32<k16 == 0>(acc[ft], wf[n % NWF], act[k16]);
      __builtin_amdgcn_sched_barrier(0);
      if constexpr ((n + PDN) % 40 == 0 && n + PDN < 280) {
        constexpr int c = (n + PDN) / 40;
        boundary(IC<cap63(c < 2 ? 63 : c == 2 ? S4.before(n) : S4.before(n) - S4.end_of(1, c, 2))>{});
      }
      if constexpr (n == SIG_A) {
        wait_vm<S4.before(n) - S4.end_of(3, 3, 0)>();
        raw_bar();
        signal(cntA, tid);
      }
      if constexpr (n + PDN < 280) rdw(IC<n + PDN>{});
      static_for<0, 2>([&](auto K) {
        constexpr int u = epi_first(n) + decltype(K)::v;
        if constexpr (u < NEPI) { if constexpr (epi_slot(u) == n) epi(IC<u>{}); }
      });
      events(N);
      __builtin_amdgcn_sched_barrier(0);
    });
    STAMP(ws, 3, 9);
    asm volatile("s_nop 7\n\ts_nop 7" : "+v"(acc[6]));
#pragma unroll
    for (int p = 0; p < 4; ++p) {
      const f32x2 g = lo2(acc[6], 2 * p), u = lo2(acc[6], 8 + 2 * p);
      const f32x2 z = g * (g * g * -0.1029432397f + -2.3022081983f);
      const f32x2 den = f32x2{__builtin_amdgcn_exp2f(z[0]), __builtin_amdgcn_exp2f(z[1])} + 1.f;
      const f32x2 ov = (g * f32x2{__builtin_amdgcn_rcpf(den[0]), __builtin_amdgcn_rcpf(den[1])}) * u;
      gout[p] = pk_bf16(ov[0], ov[1]);
    }
    gstore(6);
  }
  STAMP(ws, 3, 10);
    poll_check<S4.before(1000) + 1 - S4.end_of(2, 0, 0)>(pollv, cntA);
  boundary(IC<S4.before(1000) + 1 - S4.end_of(1, 7, 2)>{});
  STAMP(ws, 3, 11);
  {
    const int mh = w & 1, kh = w >> 1;
    u32x4 gf[28][2];
    u32x2 hv[5];
    const int mrow = 64 * mb + 32 * mh + 16 * kh + (lane & 15), ncol = 80 * nb + 4 * (lane >> 4);
    const char* gg = ws + G_OFF + (size_t)(mb * 56 + kh) * 4096 + mh * 2048;
    const u32 gv0 = voff + kh * 1024, gv1 = voff + (1 - kh) * 1024;
#pragma unroll
    for (int i = 0; i < 3; ++i) { lb[i] += kh * 5120; pin(lb[i]); }
    auto rd5 = [&](auto F) {
      constexpr int f = decltype(F)::v, o = ((280 + 10 * (f / 5)) % 160 + f % 5) * 1024;
      if constexpr (f < 140) wf[f % NWF] = *(const u32x4*)(smem + lb[o >> 16] + (o & 65535));
    };
    auto events = [&](auto N) {
      static_for<S5.first(decltype(N)::v), S5.first(decltype(N)::v + 1)>([&](auto I) {
        constexpr Ev e = S5.ev[decltype(I)::v];
        if constexpr (e.kind == 4) ld2_dev(gf[e.a][0], gf[e.a][1], gv0, gv1, gg + e.a * 8192);
        if constexpr (e.kind == 1) chunk_part(IC<e.a>{}, IC<e.b>{});
        if constexpr (e.kind == 2) poll_issue(pollv, cntB);
        if constexpr (e.kind == 5) ldh_dev(hv, (u32)(mrow * 640 + ncol) * 2u, ws + H_OFF);
      });
    };
    events(IC<-1>{});
    f32x4 acc[2][5];
    static_for<0, PDN / 2>(rd5);
    static_for<280, 560>([&](auto N) {
      constexpr int n = decltype(N)::v, j = (n - 280) / 10, nt = ((n - 280) % 10) / 2, e = n & 1, f = (n - 280) / 2;
      if constexpr ((n - 280) % 10 == 0) wait_a2<S5.before(n) - S5.end_of(4, j, 0)>(gf[j][0], gf[j][1]);
      mfma16<j == 0>(acc[e][nt], wf[f % NWF], gf[j][e]);
      __builtin_amdgcn_sched_barrier(0);
      if constexpr ((n + PDN) % 40 == 0 && n + PDN < 560) {
        constexpr int c = (n + PDN) / 40;
        boundary(IC<cap63(c < 10 ? 63 : S5.before(n) - S5.end_of(1, c, 2))>{});
      }
      if constexpr (n == SIG_B) {
        raw_bar();
        signal(cntB, tid);
      }
      if constexpr (n == CHECK_B) poll_check<S5.before(n) - S5.end_of(2, 0, 0)>(pollv, cntB);
      if constexpr (e == 0) rd5(IC<f + PDN / 2>{});
      events(N);
      __builtin_amdgcn_sched_barrier(0);
    });
    STAMP(ws, 3, 12);
    asm volatile("s_nop 7\n\ts_nop 7" : "+v"(acc[0][0]), "+v"(acc[0][1]), "+v"(acc[0][2]), "+v"(acc[0][3]), "+v"(acc[0][4]),
                 "+v"(acc[1][0]), "+v"(acc[1][1]), "+v"(acc[1][2]), "+v"(acc[1][3]), "+v"(acc[1][4]));
    f32x4* red = (f32x4*)smem + lane;
#pragma unroll
    for (int j = 0; j < 5; ++j) red[(w * 5 + j) * 64] = acc[1][j];
    asm volatile("s_waitcnt vmcnt(0) lgkmcnt(0)" : "+v"(hv[0]), "+v"(hv[1]), "+v"(hv[2]), "+v"(hv[3]), "+v"(hv[4])::"memory");
    raw_bar();
    u16* op = out + (size_t)mrow * 640 + ncol;
#pragma unroll
    for (int j = 0; j < 5; ++j) {
      const f32x4 v = acc[0][j] + red[((w ^ 2) * 5 + j) * 64];
      const u32 o0 = pk_bf16(v[0], v[1]), o1 = pk_bf16(v[2], v[3]);
      u32x2 o = {pk_bf16(bf_lo(hv[j][0]) + bf_lo(o0), bf_hi(hv[j][0]) + bf_hi(o0)),
                 pk_bf16(bf_lo(hv[j][1]) + bf_lo(o1), bf_hi(hv[j][1]) + bf_hi(o1))};
      *(u32x2*)(op + 16 * j) = o;
    }
  }
  STAMP(ws, 3, 13);
  STAMPE(ws, 3);
}

extern "C" void kernel_launch(void* const* d_in, const int* in_sizes, int n_in, void* d_out, int out_size, void* d_ws,
                              size_t ws_size, hipStream_t stream) {
  const u16* x = (const u16*)d_in[0];
  const u16* wr = (const u16*)d_in[1];
  const u16* wi = (const u16*)d_in[2];
  const u16* wg = (const u16*)d_in[4];
  const u16* wu = (const u16*)d_in[5];
  const u16* wd = (const u16*)d_in[6];
  char* ws = (char*)d_ws;
  k1_kernel<<<K1_GRID, 256, 0, stream>>>(x, wr, wi, ws);
  k2_kernel<<<K2_GRID, 256, 0, stream>>>(wg, wu, wd, ws);
  k3_kernel<<<256, 256, 0, stream>>>(x, ws, (u16*)d_out);
}
```

```cpp
#include <hip/hip_runtime.h>
#include <hip/hip_bf16.h>
#include <stdint.h>

#define DI __device__ __forceinline__
typedef unsigned short u16;
typedef uint32_t u32;
typedef _Float16 f16;
typedef f16 f16x2 __attribute__((ext_vector_type(2)));
typedef f16 f16x4 __attribute__((ext_vector_type(4)));
typedef f16 f16x8 __attribute__((ext_vector_type(8)));
typedef __bf16 bf16x2 __attribute__((ext_vector_type(2)));
typedef __bf16 bf16x8 __attribute__((ext_vector_type(8)));
typedef short s16x4 __attribute__((ext_vector_type(4)));
typedef float f32x2 __attribute__((ext_vector_type(2)));
typedef float f32x4 __attribute__((ext_vector_type(4)));
typedef float f32x16 __attribute__((ext_vector_type(16)));
typedef u32 u32x2 __attribute__((ext_vector_type(2)));
typedef u32 u32x4 __attribute__((ext_vector_type(4)));
typedef int i32x4 __attribute__((ext_vector_type(4)));
#define LAS __attribute__((address_space(3)))

constexpr int NCOLWG = 162, NSLOT = 4 * NCOLWG;
constexpr size_t X1_OFF = 0;
constexpr size_t X1_SZ = 256ull * NSLOT * 16;
constexpr size_t WT_OFF = X1_OFF + X1_SZ;
constexpr size_t WT_SZ = 323ull * 8192;
constexpr size_t X2_OFF = WT_OFF + WT_SZ;
constexpr size_t X2_SZ = 160ull * 16384;
constexpr size_t EC_OFF = X2_OFF + X2_SZ;
constexpr size_t EC_SZ = 4096;
constexpr size_t H_OFF = EC_OFF + EC_SZ;
constexpr size_t H_SZ = 2048ull * 640 * 2;
constexpr size_t ACT_OFF = H_OFF + H_SZ;
constexpr size_t ACT_SZ = 64ull * 40 * 1024;
constexpr size_t W1_OFF = ACT_OFF + ACT_SZ;
constexpr size_t W1_SZ = 16ull * 280 * 1024;
constexpr size_t W2_OFF = W1_OFF + W1_SZ;
constexpr size_t W2_SZ = 8ull * 56 * 5 * 1024;
constexpr size_t G_OFF = W2_OFF + W2_SZ;
constexpr size_t G_SZ = 32ull * 56 * 4 * 1024;
constexpr size_t CNT_OFF = G_OFF + G_SZ;

#define STAMPN(ws_, k_, i_) do {} while (0)
#define STAMPF(ws_, k_, i_) do {} while (0)
#define STAMP(ws_, k_, i_) do {} while (0)
#define STAMPE(ws_, k_) do {} while (0)
DI float asf(u32 u) { return __builtin_bit_cast(float, u); }
DI u32 asu(float f) { return __builtin_bit_cast(u32, f); }
DI u32 pk_bf16(float a, float b) { f32x2 v = {a, b}; return __builtin_bit_cast(u32, __builtin_convertvector(v, bf16x2)); }
DI u32 pk_f16(float a, float b) { f32x2 v = {a, b}; return __builtin_bit_cast(u32, __builtin_convertvector(v, f16x2)); }
DI u32 pk_f16v(f32x2 v) { return __builtin_bit_cast(u32, __builtin_convertvector(v, f16x2)); }
DI float bf_lo(u32 u) { return asf(u << 16); }
DI float bf_hi(u32 u) { return asf(u & 0xffff0000u); }
DI float bfr(float a) { return bf_lo(pk_bf16(a, a)); }
DI float cosr(float rev) { return __builtin_amdgcn_cosf(rev); }
DI float sinr(float rev) { return __builtin_amdgcn_sinf(rev); }
constexpr __host__ __device__ int crow(int reg, int h) { return (reg & 3) + 8 * (reg >> 2) + 4 * h; }
DI u32 umax(u32 a, u32 b) { return a > b ? a : b; }

template <int CTRL> DI u32 dppu(u32 v) { return (u32)__builtin_amdgcn_update_dpp(0, (int)v, CTRL, 0xf, 0xf, true); }
template <int CTRL> DI float dpp(float v) { return asf(dppu<CTRL>(asu(v))); }
DI float sum32(float v) {
  v += dpp<0xB1>(v); v += dpp<0x4E>(v); v += dpp<0x141>(v); v += dpp<0x140>(v);
  auto r = __builtin_amdgcn_permlane16_swap(asu(v), asu(v), false, false);
  return asf(r[0]) + asf(r[1]);
}
DI float sum64(float v) {
  v = sum32(v);
  auto r = __builtin_amdgcn_permlane32_swap(asu(v), asu(v), false, false);
  return asf(r[0]) + asf(r[1]);
}
DI u32 max64(u32 v) {
  v = umax(v, dppu<0xB1>(v)); v = umax(v, dppu<0x4E>(v)); v = umax(v, dppu<0x141>(v)); v = umax(v, dppu<0x140>(v));
  auto r = __builtin_amdgcn_permlane16_swap(v, v, false, false);
  v = umax(r[0], r[1]);
  auto s = __builtin_amdgcn_permlane32_swap(v, v, false, false);
  return umax(s[0], s[1]);
}
DI f16x8 asf16x8(u32x4 v) { return __builtin_bit_cast(f16x8, v); }
DI f32x16 mf16(u32x4 a, u32x4 b, f32x16 c) { return __builtin_amdgcn_mfma_f32_32x32x16_f16(asf16x8(a), asf16x8(b), c, 0, 0, 0); }
DI f32x16 mb32(u32x4 a, u32x4 b, f32x16 c) {
  return __builtin_amdgcn_mfma_f32_32x32x16_bf16(__builtin_bit_cast(bf16x8, a), __builtin_bit_cast(bf16x8, b), c, 0, 0, 0);
}
DI f32x4 mb16(u32x4 a, u32x4 b, f32x4 c) {
  return __builtin_amdgcn_mfma_f32_16x16x32_bf16(__builtin_bit_cast(bf16x8, a), __builtin_bit_cast(bf16x8, b), c, 0, 0, 0);
}
DI u32x2 tr_read(const char* p) {
  s16x4 r = __builtin_amdgcn_ds_read_tr16_b64_v4i16((LAS s16x4*)(p));
  return __builtin_bit_cast(u32x2, r);
}
DI f32x16 zero16() { f32x16 z; for (int i = 0; i < 16; ++i) z[i] = 0.f; return z; }
DI int wave_id() { return __builtin_amdgcn_readfirstlane(threadIdx.x >> 6); }
DI f32x2 lo2(const f32x16& v, int i) { return f32x2{v[i], v[i + 1]}; }
DI f32x2 pr2(const f32x4& v, int i) { return f32x2{v[i], v[i + 1]}; }
DI u32 pkmul_h(u32 a, u32 b) {
  const f16x2 r = __builtin_bit_cast(f16x2, a) * __builtin_bit_cast(f16x2, b);
  return __builtin_bit_cast(u32, r);
}
DI u32 pow2_h2(int d) {
  const float s = __builtin_amdgcn_ldexpf(1.f, d);
  return pk_f16(s, s);
}
template <class T> DI void pin(T& v) { asm volatile("" : "+v"(v)); }

DI void ln_scalars(float mean, float var, float& mb, float& inv) {
  mb = bfr(mean);
  float vb = bfr(var);
  float t = bfr(vb + 1e-5f);
  float den = bfr(__builtin_amdgcn_sqrtf(t));
  inv = __builtin_amdgcn_rcpf(den);
}

template <int N> struct IC { static constexpr int v = N; };
template <int I, int N, class F> DI void static_for(F&& f) {
  if constexpr (I < N) { f(IC<I>{}); static_for<I + 1, N>(f); }
}
template <int N> DI void wait_vm() { asm volatile("s_waitcnt vmcnt(%0)" ::"n"(N) : "memory"); }
DI void wait_lgkm0() { asm volatile("s_waitcnt lgkmcnt(0)" ::: "memory"); }
DI void raw_bar() { asm volatile("s_barrier" ::: "memory"); }
DI u32 lds_addr(const char* p) { return (u32)(size_t)(LAS const char*)p; }

template <int N> DI void dma(u32 voff, const void* g, u32 lds) {
  if constexpr (N == 4)
    asm volatile("s_mov_b32 m0, %2\n\ts_nop 0\n\tglobal_load_lds_dwordx4 %0, %1\n\t"
                 "global_load_lds_dwordx4 %0, %1 offset:1024\n\tglobal_load_lds_dwordx4 %0, %1 offset:2048\n\t"
                 "global_load_lds_dwordx4 %0, %1 offset:3072" ::"v"(voff), "s"(g), "s"(lds) : "memory");
  else if constexpr (N == 3)
    asm volatile("s_mov_b32 m0, %2\n\ts_nop 0\n\tglobal_load_lds_dwordx4 %0, %1\n\t"
                 "global_load_lds_dwordx4 %0, %1 offset:1024\n\tglobal_load_lds_dwordx4 %0, %1 offset:2048" ::"v"(voff),
                 "s"(g), "s"(lds) : "memory");
  else if constexpr (N == 2)
    asm volatile("s_mov_b32 m0, %2\n\ts_nop 0\n\tglobal_load_lds_dwordx4 %0, %1\n\t"
                 "global_load_lds_dwordx4 %0, %1 offset:1024" ::"v"(voff), "s"(g), "s"(lds) : "memory");
  else
    asm volatile("s_mov_b32 m0, %2\n\ts_nop 0\n\tglobal_load_lds_dwordx4 %0, %1" ::"v"(voff), "s"(g), "s"(lds) : "memory");
}
DI void st16_dev(u32 voff, const void* g, u32x4 v) {
  asm volatile("global_store_dwordx4 %0, %1, %2 sc1\n\ts_nop 1" ::"v"(voff), "v"(v), "s"(g) : "memory");
}
DI void st8_dev(u32 voff, const void* g, u32x2 v) {
  asm volatile("global_store_dwordx2 %0, %1, %2 sc1" ::"v"(voff), "v"(v), "s"(g) : "memory");
}
DI void ld4_dev(u32x4& a0, u32x4& a1, u32x4& a2, u32x4& a3, u32 voff, const void* g) {
  asm volatile("global_load_dwordx4 %0, %4, %5 sc1\n\tglobal_load_dwordx4 %1, %4, %5 offset:1024 sc1\n\t"
               "global_load_dwordx4 %2, %4, %5 offset:2048 sc1\n\tglobal_load_dwordx4 %3, %4, %5 offset:3072 sc1"
               : "=&a"(a0), "=&a"(a1), "=&a"(a2), "=&a"(a3) : "v"(voff), "s"(g) : "memory");
}
DI void ld1_dev(u32x4& a0, u32 voff, const void* g) {
  asm volatile("global_load_dwordx4 %0, %1, %2 sc1" : "=&a"(a0) : "v"(voff), "s"(g) : "memory");
}
DI void ldh_dev(u32x2 (&hv)[5], u32 vo, const void* g) {
  asm volatile("global_load_dwordx2 %0, %5, %6 sc1\n\tglobal_load_dwordx2 %1, %5, %6 offset:32 sc1\n\t"
               "global_load_dwordx2 %2, %5, %6 offset:64 sc1\n\tglobal_load_dwordx2 %3, %5, %6 offset:96 sc1\n\t"
               "global_load_dwordx2 %4, %5, %6 offset:128 sc1"
               : "=&v"(hv[0]), "=&v"(hv[1]), "=&v"(hv[2]), "=&v"(hv[3]), "=&v"(hv[4]) : "v"(vo), "s"(g) : "memory");
}
template <int N> DI void wait_a2(u32x4& a0, u32x4& a1) {
  asm volatile("s_waitcnt vmcnt(%2)" : "+a"(a0), "+a"(a1) : "n"(N) : "memory");
}
DI float vmul(float a, float b) { float d; asm volatile("v_mul_f32 %0, %1, %2" : "=v"(d) : "v"(a), "v"(b)); return d; }
DI float vpoly(float z, float k1) { float d; asm volatile("v_fmamk_f32 %0, %1, 0xbdd2d3e8, %2" : "=v"(d) : "v"(z), "v"(k1)); return d; }
DI float vadd1(float a) { float d; asm volatile("v_add_f32 %0, 1.0, %1" : "=v"(d) : "v"(a)); return d; }
DI float vexp(float a) { float d; asm volatile("v_exp_f32 %0, %1" : "=v"(d) : "v"(a)); return d; }
DI float vrcp(float a) { float d; asm volatile("v_rcp_f32 %0, %1" : "=v"(d) : "v"(a)); return d; }
template <bool FIRST> DI void mfma32(f32x16& d, const u32x4& a, const u32x4& b) {
  if constexpr (FIRST) asm volatile("v_mfma_f32_32x32x16_bf16 %0, %1, %2, 0" : "=&v"(d) : "v"(a), "a"(b));
  else asm volatile("v_mfma_f32_32x32x16_bf16 %0, %1, %2, %0" : "+v"(d) : "v"(a), "a"(b));
}
template <bool FIRST> DI void mfma16(f32x4& d, const u32x4& a, const u32x4& b) {
  if constexpr (FIRST) asm volatile("v_mfma_f32_16x16x32_bf16 %0, %1, %2, 0" : "=&v"(d) : "v"(a), "a"(b));
  else asm volatile("v_mfma_f32_16x16x32_bf16 %0, %1, %2, %0" : "+v"(d) : "v"(a), "a"(b));
}
DI void signal(u32* cnt, int tid) {
  if (tid == 0) __hip_atomic_fetch_add(cnt, 1u, __ATOMIC_RELAXED, __HIP_MEMORY_SCOPE_AGENT);
}
DI void poll_issue(u32& v, const u32* cnt) {
  asm volatile("global_load_dword %0, %1, %2 sc1" : "=&v"(v) : "v"(0), "s"(cnt) : "memory");
}
template <int N> DI void poll_check(u32& v, u32* cnt) {
  asm volatile("s_waitcnt vmcnt(%1)" : "+v"(v) : "n"(N) : "memory");
  if (__builtin_amdgcn_readfirstlane(v) < 16u) {
    for (int it = 0; it < (1 << 20); ++it) {
      if (__builtin_amdgcn_readfirstlane(__hip_atomic_load(cnt, __ATOMIC_RELAXED, __HIP_MEMORY_SCOPE_AGENT)) >= 16u) break;
      __builtin_amdgcn_s_sleep(1);
    }
  }
  asm volatile("" ::: "memory");
}
DI void tstamp(unsigned long long& t) { asm volatile("s_memtime %0\n\ts_waitcnt lgkmcnt(0)" : "=s"(t)::"memory"); }
#define PW_STAMP
#define PW_STAMP2
template <class F> DI void publish_and_wait(u32* cnt, int tid, int w, F&& idle) {
  asm volatile("s_waitcnt vmcnt(0) lgkmcnt(0)" ::: "memory");
  raw_bar();
  PW_STAMP2;
  if (w == 0) {
    if (tid == 0) __hip_atomic_fetch_add(cnt, 1u, __ATOMIC_RELAXED, __HIP_MEMORY_SCOPE_AGENT);
    for (int it = 0; it < (1 << 20); ++it) {
      const u32 v = __builtin_amdgcn_readfirstlane(__hip_atomic_load(cnt, __ATOMIC_RELAXED, __HIP_MEMORY_SCOPE_AGENT));
      if (v >= 16u) break;
      __builtin_amdgcn_s_sleep(1);
    }
    PW_STAMP;
  } else idle();
  raw_bar();
}

constexpr double PI_ = 3.14159265358979323846;
constexpr double ccos_(double x) {
  double t = 1, s = 1;
  for (int k = 1; k <= 16; ++k) { t *= -x * x / ((2 * k - 1) * (2 * k)); s += t; }
  return s;
}
constexpr double c2048(int k) { k &= 2047; k = k > 1024 ? 2048 - k : k; return ccos_(2 * PI_ * k / 2048); }
constexpr u32 fb(double x) { return __builtin_bit_cast(u32, (float)x); }

struct ColTask { int a1, b1, am, bm; bool valid; };
constexpr __host__ __device__ ColTask col_task(int g, int p) {
  if (g < 150) { const int a1 = g / 10 + 1, b1 = 2 * (g % 10) + p; return {a1, b1, 32 - a1, 19 - b1, true}; }
  if (g < 155) { const int b1 = 2 * (g - 150) + p; return {16, b1, 16, 19 - b1, true}; }
  if (g == 155) return {0, 0, 0, 0, p == 0};
  if (g == 156) return {0, 10, 0, 10, p == 0};
  const int b1 = 2 * (g - 157) + 1 + p;
  return {0, b1 < 10 ? b1 : 9, 0, b1 < 10 ? 20 - b1 : 11, b1 < 10};
}
struct SlotMap {
  u32 v[768];
  constexpr SlotMap() : v() {
    for (int slot = 0; slot < NSLOT; ++slot) {
      const int mir = (slot >> 1) & 1;
      const ColTask t = col_task(slot >> 2, slot & 1);
      const int q1 = t.a1 + 32 * t.b1, a = mir ? t.am : t.a1, b = mir ? t.bm : t.b1;
      const bool cj = mir ? !(q1 > 320) : (q1 > 320);
      v[slot] = (u32)(33 * b + a) | (cj ? 0x80000000u : 0u);
    }
  }
};
template <int SD> struct ThetaTab {
  u32 v[4 * 256];
  constexpr ThetaTab() : v() {
    for (int l = 0; l < 64; ++l)
      for (int n = 0; n < 8; ++n) {
        const int p = 16 * SD + (l & 15) + 32 * crow(4 * (n >> 1) + 2 * ((l >> 4) & 1) + (n & 1), l >> 5);
        v[((n >> 2) * 64 + l) * 4 + (n & 3)] = fb(c2048(p));
        v[((2 + (n >> 2)) * 64 + l) * 4 + (n & 3)] = fb(c2048(p + 1536));
      }
  }
};
struct Theta { ThetaTab<0> a; ThetaTab<1> b; };
static __device__ const __attribute__((aligned(128))) SlotMap g_smap{};
static __device__ const __attribute__((aligned(128))) Theta g_theta{};

DI void dft32_consts(float (&c)[16], float (&s)[16], int r, int h) {
  const float rf = (float)r * (1.f / 32.f), r4 = rf * (float)(4 * h);
#pragma unroll
  for (int i = 0; i < 16; ++i) { const float rev = rf * (float)crow(i, 0) + r4; c[i] = cosr(rev); s[i] = sinr(rev); }
}
DI void pack_csn(const float (&c)[16], const float (&s)[16], u32x4 (&Cp)[2], u32x4 (&Sp)[2], u32x4 (&Np)[2]) {
#pragma unroll
  for (int i = 0; i < 8; ++i) {
    Cp[i >> 2][i & 3] = pk_f16(c[2 * i], c[2 * i + 1]);
    Sp[i >> 2][i & 3] = pk_f16(s[2 * i], s[2 * i + 1]);
    Np[i >> 2][i & 3] = pk_f16(-s[2 * i], -s[2 * i + 1]);
  }
}

constexpr int K1_IMG = 1344;
constexpr int K1_PLANE = 33 * 20 * 4;
constexpr int K1_PL0 = 8 * K1_IMG;
constexpr int K1_LDS = 22528;
constexpr int K1_WT_WGS = 128;
constexpr int K1_GRID = 256 + K1_WT_WGS;
static_assert(K1_PL0 + 4 * K1_PLANE <= K1_LDS, "LDS");

DI void k1_row(const u16* __restrict__ x, char* __restrict__ X1, char* smem, int wg) {
  const int tid = threadIdx.x, lane = tid & 63, w = wave_id(), r = lane & 31, h = lane >> 5;
  const int b2 = wg >> 3, aq = wg & 7;
  const int m = 32 * (4 * aq + w) + b2;
  u32x2 v[5];
  u32 e[3];
  {
    const char* src = (const char*)x + (size_t)m * 2560;
    const u32 vo = h * 1280 + r * 8;
    asm volatile("global_load_dwordx2 %0, %5, %6\n\tglobal_load_dwordx2 %1, %5, %6 offset:256\n\t"
                 "global_load_dwordx2 %2, %5, %6 offset:512\n\tglobal_load_dwordx2 %3, %5, %6 offset:768\n\t"
                 "global_load_dwordx2 %4, %5, %6 offset:1024"
                 : "=&v"(v[0]), "=&v"(v[1]), "=&v"(v[2]), "=&v"(v[3]), "=&v"(v[4]) : "v"(vo), "s"(src) : "memory");
    asm volatile("global_load_dword %0, %3, %4\n\tglobal_load_dword %1, %3, %4 offset:1024\n\t"
                 "global_load_dword %2, %3, %4 offset:2048"
                 : "=&v"(e[0]), "=&v"(e[1]), "=&v"(e[2]) : "v"(tid * 4), "s"(g_smap.v) : "memory");
  }
  u32x4 C1[2], S1[2], N1[2], C2, S2, N2, MR, MI;
  f32x2 tc[6], ts[6];
  {
    float c[16], s[16];
    dft32_consts(c, s, r, h);
    pack_csn(c, s, C1, S1, N1);
    const float rf = (float)r * (1.f / 640.f), r4 = rf * (float)(4 * h);
#pragma unroll
    for (int i = 0; i < 12; ++i) {
      const float rev = rf * (float)crow(i, 0) + r4;
      tc[i >> 1][i & 1] = cosr(rev); ts[i >> 1][i & 1] = sinr(rev);
      const float rev2 = rev * 32.f;
      c[i] = cosr(rev2); s[i] = sinr(rev2);
    }
#pragma unroll
    for (int i = 0; i < 4; ++i) {
      C2[i] = pk_f16(c[2 * i], c[2 * i + 1]); S2[i] = pk_f16(s[2 * i], s[2 * i + 1]); N2[i] = pk_f16(-s[2 * i], -s[2 * i + 1]);
    }
#pragma unroll
    for (int i = 0; i < 2; ++i) {
      const u32 cc = h ? 0u : pk_f16(c[8 + 2 * i], c[9 + 2 * i]), ss = h ? 0u : pk_f16(s[8 + 2 * i], s[9 + 2 * i]);
      MR[i] = cc; MR[2 + i] = ss; MI[i] = ss ^ 0x80008000u; MI[2 + i] = cc;
    }
#pragma unroll
    for (int i = 0; i < 2; ++i) { pin(C1[i]); pin(S1[i]); pin(N1[i]); }
    pin(C2); pin(S2); pin(N2); pin(MR); pin(MI);
#pragma unroll
    for (int i = 0; i < 6; ++i) { pin(tc[i]); pin(ts[i]); }
  }
  STAMP(X1 - X1_OFF, 1, 5);
  asm volatile("s_waitcnt vmcnt(3)" : "+v"(v[0]), "+v"(v[1]), "+v"(v[2]), "+v"(v[3]), "+v"(v[4])::"memory");
  STAMP(X1 - X1_OFF, 1, 1);
  f32x2 f[10];
#pragma unroll
  for (int i = 0; i < 5; ++i) {
    f[2 * i] = f32x2{bf_lo(v[i][0]), bf_hi(v[i][0])};
    f[2 * i + 1] = f32x2{bf_lo(v[i][1]), bf_hi(v[i][1])};
  }
  f32x2 s2 = f[0];
#pragma unroll
  for (int i = 1; i < 10; ++i) s2 += f[i];
  const float mean = sum32(s2[0] + s2[1]) * (1.f / 640.f);
  f32x2 q2 = {0.f, 0.f};
#pragma unroll
  for (int i = 0; i < 10; ++i) { const f32x2 d = f[i] - mean; q2 += d * d; }
  const float var = sum32(q2[0] + q2[1]) * (1.f / 640.f);
  float mb, inv;
  ln_scalars(mean, var, mb, inv);
  char* img = smem + w * (2 * K1_IMG);
#pragma unroll
  for (int i = 0; i < 5; ++i) {
    u32x2 o;
#pragma unroll
    for (int k = 0; k < 2; ++k) {
      const f32x2 d0 = f[2 * i + k] - mb;
      const u32 d = pk_bf16(d0[0], d0[1]);
      const f32x2 n0 = f32x2{bf_lo(d), bf_hi(d)} * inv;
      const u32 n = pk_bf16(n0[0], n0[1]);
      o[k] = pk_f16(bf_lo(n), bf_hi(n));
    }
    *(u32x2*)(img + h * K1_IMG + 8 * (r + 32 * i)) = o;
  }
  if (r < 8) { u32x2 z = {0u, 0u}; *(u32x2*)(img + h * K1_IMG + 1280 + 8 * r) = z; }
  asm volatile("" ::: "memory");
  STAMP(X1 - X1_OFF, 1, 2);
  const int q4 = (lane & 15) >> 2, p4 = lane & 3, blk = (lane >> 4) & 1;
  const char* tb = img + (4 * h + q4) * 40 + 32 * blk + 8 * p4;
  f32x16 Dr = zero16(), Di = zero16();
#pragma unroll
  for (int s = 0; s < 2; ++s) {
    u32x2 r0 = tr_read(tb + 640 * s), r1 = tr_read(tb + 640 * s + 320);
    u32x2 i0 = tr_read(tb + K1_IMG + 640 * s), i1 = tr_read(tb + K1_IMG + 640 * s + 320);
    u32x4 ar = {r0[0], r0[1], r1[0], r1[1]}, ai = {i0[0], i0[1], i1[0], i1[1]};
    Dr = mf16(ar, C1[s], Dr); Di = mf16(ai, C1[s], Di);
    Dr = mf16(ai, S1[s], Dr); Di = mf16(ar, N1[s], Di);
  }
  u32 er[6], ei[6];
#pragma unroll
  for (int k = 0; k < 6; ++k) {
    const f32x2 dr = lo2(Dr, 2 * k), di = lo2(Di, 2 * k);
    er[k] = pk_f16v(dr * tc[k] + di * ts[k]);
    ei[k] = pk_f16v(di * tc[k] - dr * ts[k]);
  }
  const u32x4 pr = {er[0], er[1], er[2], er[3]}, pi = {ei[0], ei[1], ei[2], ei[3]}, pm = {er[4], er[5], ei[4], ei[5]};
  f32x16 Xr = zero16(), Xi = zero16();
  Xr = mf16(C2, pr, Xr); Xi = mf16(N2, pr, Xi);
  Xr = mf16(S2, pi, Xr); Xi = mf16(C2, pi, Xi);
  Xr = mf16(MR, pm, Xr); Xi = mf16(MI, pm, Xi);
  char* planes = smem + K1_PL0;
  u32* pl = (u32*)(planes + w * K1_PLANE) + 33 * 4 * h + r;
#pragma unroll
  for (int i = 0; i < 8; ++i) pl[33 * crow(i, 0)] = pk_f16(Xr[i], Xi[i]);
  if (h == 0) {
#pragma unroll
    for (int i = 8; i < 12; ++i) pl[33 * crow(i, 0)] = pk_f16(Xr[i], Xi[i]);
  }
  STAMP(X1 - X1_OFF, 1, 3);
  asm volatile("s_waitcnt vmcnt(0) lgkmcnt(0)" : "+v"(e[0]), "+v"(e[1]), "+v"(e[2])::"memory");
  raw_bar();
  STAMP(X1 - X1_OFF, 1, 4);
  u32x4* dst = (u32x4*)(X1 + (size_t)(wg >> 1) * (NSLOT * 32)) + (aq & 1) * 4 + (tid >> 2) * 8 + (tid & 3);
#pragma unroll
  for (int it = 0; it < 3; ++it) {
    if (it < 2 || tid < NSLOT - 512) {
      const u32* sp = (const u32*)planes + (e[it] & 0xffffu);
      const u32 cj = e[it] & 0x80000000u;
      u32x4 o = {sp[0] ^ cj, sp[K1_PLANE / 4] ^ cj, sp[2 * (K1_PLANE / 4)] ^ cj, sp[3 * (K1_PLANE / 4)] ^ cj};
      dst[512 * it] = o;
    }
  }
}

DI int wt_off(int i, int h, int a) { return (((a >> 4) * 4 + (i >> 1)) * 64 + h * 32 + (i & 1) * 16 + (a & 15)) * 16; }

DI void k1_wt(const u16* __restrict__ wr, const u16* __restrict__ wi, char* __restrict__ WT, char* smem, int wg) {
  const int tid = threadIdx.x, lane = tid & 63, w = wave_id();
  const int qh = wg & 1, ao = (wg >> 1) & 3, h = (wg >> 3) & 1, i = wg >> 4;
  const int p0 = 8 * ao + 32 * (crow(2 * i, 0) + 4 * h), q0 = 161 * qh, nq = 161 - qh;
  {
    const char* src = (const char*)(w >> 1 ? wi : wr);
    const u32 lds = lds_addr(smem) + (w >> 1) * 11264;
#pragma unroll
    for (int n = 0; n < 6; ++n) {
      const int ins = (w & 1) * 6 + n;
      if (ins < 11) {
        const int e = ins * 64 + lane, row = e / 22, c = e - 22 * row;
        const int p = p0 + (row & 7) + ((row >> 3) & 1) * 1024 + (row >> 4) * 32;
        const u32 vo = min((u32)((642 * p + 2 * q0) & ~15) + 16u * c, 2048u * 642u - 16u);
        dma<1>(vo, src, lds + ins * 1024);
      }
    }
  }
  wait_vm<0>();
  raw_bar();
  const int alo = tid & 7, a = 8 * ao + alo;
  const char* lb = smem + alo * 352 + 2 * ((alo + q0) & 7) + 2 * (tid >> 3);
  char* ob = WT + (size_t)(q0 + (tid >> 3)) * 8192 + wt_off(i, h, a);
#pragma unroll
  for (int it = 0; it < 6; ++it) {
    const int ql = (tid >> 3) + 32 * it;
    if (ql < nq) {
      u32x4 o;
#pragma unroll
      for (int d = 0; d < 4; ++d)
        o[d] = (u32) * (const u16*)(lb + 64 * it + d * 2816) | ((u32) * (const u16*)(lb + 64 * it + d * 2816 + 11264) << 16);
      *(u32x4*)(ob + (size_t)it * (32 * 8192)) = o;
      if (it == 0 || it == 4) {
        const int q = q0 + ql;
        if (q == 0 || q == 320) {
          char* mb = WT + (size_t)(q ? 322 : 321) * 8192;
#pragma unroll
          for (int d = 0; d < 4; ++d) {
            const int k = p0 + alo + (d & 1) * 1024 + (d >> 1) * 32, kp = (2048 - k) & 2047;
            const int pp = kp & 1023, half = kp >> 10, a2 = pp & 31, bb = pp >> 5;
            const int e = (bb & 3) + 4 * (bb >> 3);
            *(u32*)(mb + wt_off(e >> 1, (bb >> 2) & 1, a2) + (e & 1) * 8 + half * 4) = o[d];
          }
        }
      }
    }
  }
}

DI void touch_lines(const char* g, int n, u32 limit, u32 lds) {
  for (int i = 0; i < n; ++i)
    asm volatile("s_mov_b32 m0, %2\n\ts_nop 0\n\tglobal_load_lds_dword %0, %1" ::"v"(min((u32)(i * 256 + threadIdx.x) * 128u, limit)),
                 "s"(g), "s"(lds) : "memory");
}
constexpr int K1_WARM = 8;
__global__ __launch_bounds__(256, 2) void k1_kernel(const u16* __restrict__ x, const u16* __restrict__ wr,
                                                    const u16* __restrict__ wi, char* __restrict__ ws) {
  __shared__ __attribute__((aligned(16))) char smem[K1_LDS];
  const int bid = blockIdx.x;
  STAMP(ws, 1, 0);
  if (bid < 256) k1_row(x, ws + X1_OFF, smem, (bid & 7) * 32 + (bid >> 3));
  else if (bid >= K1_GRID) {
    {
      const char *c2, *c3;
      asm volatile("s_getpc_b64 s[20:21]\n\ts_add_u32 s20, s20, _Z9k2_kernelPKtS0_S0_Pc@rel32@lo+4\n\t"
                   "s_addc_u32 s21, s21, _Z9k2_kernelPKtS0_S0_Pc@rel32@hi+12\n\ts_mov_b64 %0, s[20:21]\n\t"
                   "s_getpc_b64 s[20:21]\n\ts_add_u32 s20, s20, _Z9k3_kernelPKtPcPt@rel32@lo+4\n\t"
                   "s_addc_u32 s21, s21, _Z9k3_kernelPKtPcPt@rel32@hi+12\n\ts_mov_b64 %1, s[20:21]"
                   : "=s"(c2), "=s"(c3)::"s20", "s21", "scc");
      touch_lines(c2, 1, 16u * 1024u, lds_addr(smem));
      touch_lines(c3, 1, 24u * 1024u, lds_addr(smem));
      touch_lines((const char*)&g_theta, 1, 8u * 1024u - 4u, lds_addr(smem));
    }
  } else {
    if (bid == 256 && threadIdx.x < 48) *(u32*)(ws + CNT_OFF + threadIdx.x * 256) = 0u;
    k1_wt(wr, wi, ws + WT_OFF, smem, bid - 256);
  }
  STAMP(ws, 1, 13);
  STAMPE(ws, 1);
}

constexpr int K2_X = 0, K2_ST = 0, K2_W = 16384, K2_TH = 32768, K2_E1 = 40960, K2_E2 = 49152, K2_EB = 57344;
constexpr int K2_LDS = 80 * 1024;
constexpr int K2_GRID = 256;
static_assert(K2_EB + 64 <= K2_LDS, "LDS");

DI void k2_col(const char* __restrict__ X1, const char* __restrict__ WT, char* __restrict__ X2, unsigned char* __restrict__ ecol,
               char* smem, int wg) {
  const int tid = threadIdx.x, lane = tid & 63, w = wave_id(), r = lane & 31, h = lane >> 5;
  const int pw = w & 1, sd = w >> 1, rho = (lane >> 4) & 1;
  const u32 lds0 = lds_addr(smem);
  const u32 l16 = lane * 16;
  {
    const char* xb = X1 + (size_t)wg * 128 + (size_t)w * (NSLOT * 32);
#pragma unroll
    for (int n = 0; n < 4; ++n) {
      const int rr = n * 8 + (lane >> 3);
      const u32 vo = (u32)rr * (u32)(4 * NSLOT * 32) + (u32)(((lane & 7) ^ ((rr >> 1) & 7)) * 16);
      dma<1>(vo, xb, lds0 + K2_X + (w * 4 + n) * 1024);
    }
  }
  const ColTask tk = col_task(wg, pw);
  const int a1 = tk.a1, b1 = tk.b1, am = tk.am, bm = tk.bm;
  const bool valid = tk.valid;
  const bool direct = wg >= 155, self = wg == 155 || wg == 156;
  const int q1 = a1 + 32 * b1, qm = am + 32 * bm;
  const bool swp = q1 > 320;
  const int task = (self && pw) ? (wg == 155 ? 321 : 322) : (swp ? qm : q1);
  dma<4>(l16, WT + (size_t)task * 8192 + sd * 4096, lds0 + K2_W + (pw * 2 + sd) * 4096);
  dma<2>(l16, (const char*)&g_theta + w * 2048, lds0 + K2_TH + w * 2048);
  u32x4 BR[4], BI[4], Cp[2], Sp[2], Np[2];
  f32x2 tc[8], ts[8];
  {
    float c[16], s[16];
    dft32_consts(c, s, r, h);
    pack_csn(c, s, Cp, Sp, Np);
#pragma unroll
    for (int i = 0; i < 16; ++i) { BR[i >> 2][i & 3] = pk_f16(c[i], s[i]); BI[i >> 2][i & 3] = pk_f16(-s[i], c[i]); }
    const float rf = (float)r * (1.f / 1024.f), r4 = rf * (float)(4 * h);
#pragma unroll
    for (int i = 0; i < 16; ++i) {
      const float rev = rf * (float)crow(i, 0) + r4;
      tc[i >> 1][i & 1] = cosr(rev); ts[i >> 1][i & 1] = sinr(rev);
    }
#pragma unroll
    for (int i = 0; i < 4; ++i) { pin(BR[i]); pin(BI[i]); }
#pragma unroll
    for (int i = 0; i < 2; ++i) { pin(Cp[i]); pin(Sp[i]); pin(Np[i]); }
#pragma unroll
    for (int i = 0; i < 8; ++i) { pin(tc[i]); pin(ts[i]); }
  }
  STAMPN(X2 - X2_OFF, 2, 9);
  wait_vm<6>();
  raw_bar();
  STAMPF(X2 - X2_OFF, 2, 9);
  STAMP(X2 - X2_OFF, 2, 1);
  const int slot = (((sd == 1) != swp) ? 2 : 0) + pw;
  f32x16 Zr = zero16(), Zi = zero16();
  {
    const char* xr = smem + K2_X + r * 128 + (((h * 4 + slot) ^ ((r >> 1) & 7)) * 16);
#pragma unroll
    for (int ks = 0; ks < 4; ++ks) {
      const u32x4 a = *(const u32x4*)(xr + ks * 4096);
      Zr = mf16(a, BR[ks], Zr); Zi = mf16(a, BI[ks], Zi);
    }
  }
  STAMP(X2 - X2_OFF, 2, 2);
  u32x4 P[4];
#pragma unroll
  for (int k = 0; k < 8; ++k) {
    const f32x2 dr = lo2(Zr, 2 * k), di = lo2(Zi, 2 * k);
    P[k >> 2][k & 3] = pk_f16v(dr * tc[k] + di * ts[k]);
    P[2 + (k >> 2)][k & 3] = pk_f16v(di * tc[k] - dr * ts[k]);
  }
  {
    u32x4* e1w = (u32x4*)(smem + K2_E1 + w * 2048) + (h * 16 + (lane & 15));
    const u32x4* e1r = (const u32x4*)(smem + K2_E1 + (w ^ 2) * 2048) + (h * 16 + (lane & 15));
    if (rho != sd) {
#pragma unroll
      for (int f = 0; f < 4; ++f) e1w[32 * f] = P[f];
    }
    wait_vm<0>();
    wait_lgkm0();
    raw_bar();
    if (rho != sd) {
#pragma unroll
      for (int f = 0; f < 4; ++f) P[f] = e1r[32 * f];
    }
  }
  STAMP(X2 - X2_OFF, 2, 3);
  Zr = zero16(); Zi = zero16();
#pragma unroll
  for (int s = 0; s < 2; ++s) {
    Zr = mf16(Cp[s], P[s], Zr); Zi = mf16(Cp[s], P[2 + s], Zi);
    Zr = mf16(Sp[s], P[2 + s], Zr); Zi = mf16(Np[s], P[s], Zi);
  }
  f32x2 alr[4], ali[4], ber[4], bei[4], dcr[4], dci[4];
  {
    const u32x4* wl = (const u32x4*)(smem + K2_W + (pw * 2 + sd) * 4096) + lane;
    const u32x4* wsl = (const u32x4*)(smem + K2_W + (2 + sd) * 4096) + lane;
    const f32x4* th = (const f32x4*)(smem + K2_TH + sd * 4096) + lane;
#pragma unroll
    for (int g = 0; g < 4; ++g) {
      const u32x4 t = wl[64 * g];
      f32x2 w0r = {bf_lo(t[0]), bf_lo(t[2])}, w0i = {bf_hi(t[0]), bf_hi(t[2])};
      f32x2 w1r = {bf_lo(t[1]), bf_lo(t[3])}, w1i = {bf_hi(t[1]), bf_hi(t[3])};
      if (self) {
        const u32x4 u = wsl[64 * g];
        w0r = (w0r + f32x2{bf_lo(u[0]), bf_lo(u[2])}) * 0.5f; w0i = (w0i - f32x2{bf_hi(u[0]), bf_hi(u[2])}) * 0.5f;
        w1r = (w1r + f32x2{bf_lo(u[1]), bf_lo(u[3])}) * 0.5f; w1i = (w1i - f32x2{bf_hi(u[1]), bf_hi(u[3])}) * 0.5f;
      }
      const f32x4 thc = th[64 * (g >> 1)], ths = th[64 * (2 + (g >> 1))];
      const f32x2 ct = pr2(thc, 2 * (g & 1)), st = pr2(ths, 2 * (g & 1));
      const f32x2 sr = w0r + w1r, si = w0i + w1i, dr = w0r - w1r, di = w0i - w1i;
      alr[g] = sr - dr * st; ali[g] = si - di * st; ber[g] = sr + dr * st; bei[g] = si + di * st;
      dcr[g] = dr * ct; dci[g] = di * ct;
    }
  }
  STAMP(X2 - X2_OFF, 2, 4);
  f32x2 nur[4], nui[4], nvr[4], nvi[4];
  float mx = 0.f;
#pragma unroll
  for (int g = 0; g < 4; ++g) {
    f32x2 ur, ui, vr, vi;
#pragma unroll
    for (int k = 0; k < 2; ++k) {
      auto sr_ = __builtin_amdgcn_permlane16_swap(asu(Zr[4 * g + k]), asu(Zr[4 * g + 2 + k]), false, false);
      auto si_ = __builtin_amdgcn_permlane16_swap(asu(Zi[4 * g + k]), asu(Zi[4 * g + 2 + k]), false, false);
      ur[k] = asf(sr_[0]); vr[k] = asf(sr_[1]); ui[k] = asf(si_[0]); vi[k] = asf(si_[1]);
    }
    nur[g] = alr[g] * ur - ali[g] * ui - dcr[g] * vi - dci[g] * vr;
    nui[g] = alr[g] * ui + ali[g] * ur + dcr[g] * vr - dci[g] * vi;
    nvr[g] = ber[g] * vr - bei[g] * vi + dcr[g] * ui + dci[g] * ur;
    nvi[g] = ber[g] * vi + bei[g] * vr - dcr[g] * ur + dci[g] * ui;
#pragma unroll
    for (int k = 0; k < 2; ++k)
      mx = fmaxf(fmaxf(mx, fmaxf(fabsf(nur[g][k]), fabsf(nui[g][k]))), fmaxf(fabsf(nvr[g][k]), fabsf(nvi[g][k])));
  }
  const int eb = (int)umax(max64(asu(mx)) >> 23, 4u);
  const float sc = asf((u32)(258 - eb) << 23);
#pragma unroll
  for (int g = 0; g < 4; ++g) {
    auto qr = __builtin_amdgcn_permlane16_swap(pk_f16v(nur[g] * sc), pk_f16v(nvr[g] * sc), false, false);
    auto qi = __builtin_amdgcn_permlane16_swap(pk_f16v(nui[g] * sc), pk_f16v(nvi[g] * sc), false, false);
    P[g >> 1][2 * (g & 1)] = qr[0]; P[g >> 1][2 * (g & 1) + 1] = qr[1];
    P[2 + (g >> 1)][2 * (g & 1)] = qi[0]; P[2 + (g >> 1)][2 * (g & 1) + 1] = qi[1];
  }
  STAMP(X2 - X2_OFF, 2, 5);
  Zr = zero16(); Zi = zero16();
#pragma unroll
  for (int s = 0; s < 2; ++s) {
    Zr = mf16(P[s], Cp[s], Zr); Zi = mf16(P[s], Sp[s], Zi);
    Zr = mf16(P[2 + s], Np[s], Zr); Zi = mf16(P[2 + s], Cp[s], Zi);
  }
  int em;
  auto finish = [&](auto SD) {
    constexpr int S = decltype(SD)::v, KO = 8 * S, SO = 8 - KO;
    u32x4 kr, ki, orr, oi;
    u32x4* e2w = (u32x4*)(smem + K2_E2 + w * 2048) + lane;
    const u32x4* e2r = (const u32x4*)(smem + K2_E2 + (w ^ 2) * 2048) + lane;
    int* ebx = (int*)(smem + K2_EB);
    if (lane == 0) ebx[w] = eb;
#pragma unroll
    for (int k = 0; k < 4; ++k) {
      const f32x2 dr = lo2(Zr, SO + 2 * k), di = lo2(Zi, SO + 2 * k), c = tc[4 * S + k], sn = ts[4 * S + k];
      orr[k] = pk_f16v(dr * c - di * sn); oi[k] = pk_f16v(di * c + dr * sn);
    }
    e2w[0] = orr; e2w[64] = oi;
#pragma unroll
    for (int k = 0; k < 4; ++k) {
      const f32x2 dr = lo2(Zr, KO + 2 * k), di = lo2(Zi, KO + 2 * k), c = tc[4 * S + k], sn = ts[4 * S + k];
      kr[k] = pk_f16v(dr * c - di * sn); ki[k] = pk_f16v(di * c + dr * sn);
    }
    wait_lgkm0();
    raw_bar();
    orr = e2r[0]; oi = e2r[64];
    const int eo = __builtin_amdgcn_readfirstlane(ebx[w ^ 2]);
    const int ebu = __builtin_amdgcn_readfirstlane(eb);
    em = max(ebu, eo);
    if (ebu != eo) {
      const u32 fk = pow2_h2(ebu - em), fo = pow2_h2(eo - em);
#pragma unroll
      for (int i = 0; i < 4; ++i) { kr[i] = pkmul_h(kr[i], fk); ki[i] = pkmul_h(ki[i], fk); orr[i] = pkmul_h(orr[i], fo); oi[i] = pkmul_h(oi[i], fo); }
    }
    Zr = zero16(); Zi = zero16();
    Zr = mf16(Cp[S], kr, Zr); Zi = mf16(Sp[S], kr, Zi);
    Zr = mf16(Np[S], ki, Zr); Zi = mf16(Cp[S], ki, Zi);
    Zr = mf16(Cp[1 - S], orr, Zr); Zi = mf16(Sp[1 - S], orr, Zi);
    Zr = mf16(Np[1 - S], oi, Zr); Zi = mf16(Cp[1 - S], oi, Zi);
  };
  if (sd) finish(IC<1>{}); else finish(IC<0>{});
  const bool prim = (sd == 1) == swp;
  const int ao = prim ? a1 : am, bo = prim ? b1 : bm;
  const u32 cj = sd ? 0x80000000u : 0u;
  if (valid && lane == 0) ecol[ao * 20 + bo] = (unsigned char)em;
  STAMP(X2 - X2_OFF, 2, 7);
  if (!direct) {
    u32x4* su = (u32x4*)(smem + K2_ST) + (((prim ? 0 : 2) + (bo & 1)) * 4) * 64 + lane;
#pragma unroll
    for (int g = 0; g < 4; ++g) {
      u32x4 o = {pk_f16(Zr[4 * g], Zi[4 * g]) ^ cj, pk_f16(Zr[4 * g + 1], Zi[4 * g + 1]) ^ cj,
                 pk_f16(Zr[4 * g + 2], Zi[4 * g + 2]) ^ cj, pk_f16(Zr[4 * g + 3], Zi[4 * g + 3]) ^ cj};
      su[64 * g] = o;
    }
    wait_lgkm0();
    raw_bar();
    STAMP(X2 - X2_OFF, 2, 8);
    const int c2 = b1 >> 1;
#pragma unroll
    for (int set = 0; set < 2; ++set) {
      const int duo = set ? (am * 10 + 9 - c2) : (a1 * 10 + c2);
      const u32x4* sp = (const u32x4*)(smem + K2_ST) + (set * 8 + w) * 64 + lane;
      const u32x4 p0 = sp[0], p1 = sp[256];
      u32x2* dp = (u32x2*)(X2 + (size_t)(duo >> 1) * 16384) + (duo & 1) + 2 * (32 * (8 * w + 4 * h) + r);
#pragma unroll
      for (int i = 0; i < 4; ++i) { u32x2 o = {p0[i], p1[i]}; dp[64 * i] = o; }
    }
  } else if (valid && !(self && sd)) {
    u32* du = (u32*)(X2 + (size_t)(bo >> 2) * 16384) + (bo & 3) + 4 * (128 * h + r);
#pragma unroll
    for (int i = 0; i < 16; ++i) du[128 * crow(i, 0)] = pk_f16(Zr[i], Zi[i]) ^ cj;
  }
}

constexpr int K2_W1_MAT = 19 * 1024 + 128;
DI void k2_w1(const u16* __restrict__ wg_, const u16* __restrict__ wu_, char* __restrict__ W1, char* smem, int idx) {
  const int lane = threadIdx.x & 63, w = wave_id();
  const int t = idx >> 2, mat = w >> 1;
  const char* src = (const char*)(mat ? wu_ : wg_);
  const u32 lds0 = lds_addr(smem);
#pragma unroll
  for (int un = 0; un < 2; ++un) {
    const int u = 2 * (idx & 3) + un;
#pragma unroll
    for (int n = 0; n < 10; ++n) {
      const int ins = min((w & 1) * 10 + n, 18);
      const int e = min(ins * 64 + lane, 1199), row = e / 15, c = e - 15 * row;
      const u32 vo = min((u32)((80 * u + row) * 3456 + (216 * t & ~15) + 16 * c), 640u * 3456u - 16u);
      dma<1>(vo, src, lds0 + (un * 2 + mat) * K2_W1_MAT + ins * 1024);
    }
  }
  const int h2_ = lane >> 5, blk = (lane >> 4) & 1, q4 = (lane & 15) >> 2, p4 = lane & 3;
#pragma unroll
  for (int un = 0; un < 2; ++un) {
    const int u = 2 * (idx & 3) + un;
    if (un == 0) wait_vm<10>(); else wait_vm<9>();
    raw_bar();
    const char* lb = smem + (un * 2 + blk) * K2_W1_MAT + (t & 1) * 8 + 8 * p4;
    u32x4 o[9];
#pragma unroll
    for (int i = 0; i < 9; ++i) {
      const int fr = min(w + 4 * i, 34), kk = fr / 7, ft = fr - 7 * kk;
      const int r0 = kk < 4 ? 20 * kk + 4 * h2_ + q4 : 40 * h2_ + 16 + q4;
      const int r1 = kk < 4 ? r0 + 8 : r0 + 20;
      const u32x2 lo = tr_read(lb + r0 * 240 + 32 * ft), hi = tr_read(lb + r1 * 240 + 32 * ft);
      o[i] = u32x4{lo[0], lo[1], hi[0], hi[1]};
    }
#pragma unroll
    for (int i = 0; i < 9; ++i) {
      const int fr = min(w + 4 * i, 34), kk = fr / 7, ft = fr - 7 * kk;
      const int k16 = kk < 4 ? 4 * u + kk : 32 + u;
      const int pos = ft < 3 ? k16 * 3 + ft : 40 * ft + k16;
      if (ft == 6 && (lane & 15) >= 12) o[i] = u32x4{0u, 0u, 0u, 0u};
      *((u32x4*)(W1 + ((size_t)t * 280 + pos) * 1024) + lane) = o[i];
    }
  }
}
DI void k2_w2(const u16* __restrict__ wd, char* __restrict__ W2, char* smem, int idx) {
  const int lane = threadIdx.x & 63, w = wave_id();
  const u32 lds0 = lds_addr(smem);
#pragma unroll
  for (int un = 0; un < 2; ++un) {
    const int k32 = 2 * idx + un;
#pragma unroll
    for (int n = 0; n < 10; ++n) {
      const int e = (w * 10 + n) * 64 + lane, row = e / 80, c = e - 80 * row, e2 = row >> 4;
      const int t = k32 < 32 ? k32 >> 1 : (k32 < 48 ? k32 - 32 : 2 * (k32 - 48) + e2);
      const int ft = k32 < 32 ? 2 * (k32 & 1) + e2 : (k32 < 48 ? 4 + e2 : 6), fl = min(16 * ft + (row & 15), 107);
      dma<1>((u32)((108 * t + fl) * 1280 + ((c ^ ((row & 7) << 1)) * 16)), wd, lds0 + un * 40960 + (w * 10 + n) * 1024);
    }
  }
  const int g = lane >> 4, q4 = (lane & 15) >> 2, p4 = lane & 3;
  const int row = 16 * (g >> 1) + 4 * (g & 1) + q4, sw = (row & 7) << 1;
#pragma unroll
  for (int un = 0; un < 2; ++un) {
    const int k32 = 2 * idx + un;
    wait_vm<10>();
    raw_bar();
    const char* lb = smem + un * 40960 + row * 1280 + (p4 & 1) * 8;
#pragma unroll
    for (int i = 0; i < 10; ++i) {
      const int fr = w * 10 + i;
      const char* a = lb + (((2 * fr + (p4 >> 1)) ^ sw) * 16);
      u32x2 lo = tr_read(a), hi = tr_read(a + 8 * 1280);
      if (k32 >= 48 && (g & 1)) hi = u32x2{0u, 0u};
      const int nb = fr / 5, nt = fr - 5 * nb;
      *((u32x4*)(W2 + ((size_t)(nb * 56 + k32) * 5 + nt) * 1024) + lane) = u32x4{lo[0], lo[1], hi[0], hi[1]};
    }
  }
}
static_assert(4 * K2_W1_MAT <= K2_LDS && 81920 <= K2_LDS, "LDS");

__global__ __launch_bounds__(256, 2) void k2_kernel(const u16* __restrict__ wg_, const u16* __restrict__ wu_,
                                                    const u16* __restrict__ wd, char* __restrict__ ws) {
  __shared__ __attribute__((aligned(16))) char smem[K2_LDS];
  const int bid = blockIdx.x;
  STAMP(ws, 2, 0);
  const int lw = bid < 168 ? (bid & 7) * 21 + (bid >> 3) : bid;
  if (lw < NCOLWG) k2_col(ws + X1_OFF, ws + WT_OFF, ws + X2_OFF, (unsigned char*)(ws + EC_OFF), smem, lw);
  else if (lw < NCOLWG + 64) k2_w1(wg_, wu_, ws + W1_OFF, smem, lw - NCOLWG);
  else if (lw < NCOLWG + 92) k2_w2(wd, ws + W2_OFF, smem, lw - NCOLWG - 64);
  STAMP(ws, 2, 13);
  STAMPE(ws, 2);
}

constexpr int R_BASE = 120 * 1024;
constexpr int R_X2 = R_BASE, R_EC = R_BASE + 10240, R_ST = R_BASE + 12288, R_H = R_ST + 80 * 144;
static_assert(R_H + 10240 <= 160 * 1024, "LDS");
constexpr int PDN = 10, NWF = 12;
constexpr int bnd_slot(int c) { return 40 * c - PDN; }
constexpr int NEPI = 192;
constexpr int epi_slot(int u) { return u < 128 ? 121 + (u * 94) / 128 : 216 + ((u - 128) * 63) / 64; }
constexpr int epi_first(int n) { int u = 0; while (u < NEPI && epi_slot(u) < n) ++u; return u; }
constexpr int gst_slot(int e) { return epi_slot(32 * e + 31) + 1; }
constexpr int SIG_A = 248, POLL_A = 270;
constexpr int SIG_B = 280 + 5 * 2, POLL_B = 280 + 5 * 15, CHECK_B = 280 + 5 * 20;
struct Ev { int slot, kind, a, b; };
constexpr int ev_size(const Ev& e) { return e.kind == 0 ? 4 : e.kind == 1 ? (e.b == 0 ? 4 : 3) : e.kind == 5 ? 5 : 1; }
template <int N> struct Sched {
  Ev ev[N];
  int n;
  constexpr void add(int slot, int kind, int a, int b) {
    int i = n++;
    while (i > 0 && ev[i - 1].slot > slot) { ev[i] = ev[i - 1]; --i; }
    ev[i] = Ev{slot, kind, a, b};
  }
  constexpr int first(int slot) const { int i = 0; while (i < n && ev[i].slot < slot) ++i; return i; }
  constexpr int before(int slot) const { int v = 0; for (int i = 0; i < n; ++i) v += ev[i].slot < slot ? ev_size(ev[i]) : 0; return v; }
  constexpr int end_of(int kind, int a, int b) const {
    int v = 0;
    for (int i = 0; i < n; ++i) { v += ev_size(ev[i]); if (ev[i].kind == kind && ev[i].a == a && ev[i].b == b) return v; }
    return -1000;
  }
};
constexpr Sched<44> make_s4() {
  Sched<44> s{};
  for (int g = 0; g < 10; ++g) s.add(g < 3 ? -1 : 12 * (g - 3) + 1, 0, g, 0);
  const int c0[7] = {80, 100, 124, 151, 181, 205, 232}, dp[7] = {6, 8, 9, 9, 8, 7, 6};
  for (int c = 0; c < 7; ++c) for (int p = 0; p < 3; ++p) s.add(c0[c] + dp[c] * p, 1, 3 + c, p);
  for (int e = 0; e < 6; ++e) s.add(gst_slot(e), 3, e, 0);
  s.add(POLL_A, 2, 0, 0);
  return s;
}
constexpr Sched<76> make_s5() {
  Sched<76> s{};
  for (int k = 0; k < 56; ++k) s.add(k < 12 ? -1 : 280 + 5 * (k - 12) + 1, 4, k, 0);
  for (int c = 0; c < 4; ++c) for (int p = 0; p < 3; ++p) s.add(280 + 5 * (4 + 8 * c + 2 * p) + 3, 1, 10 + c, p);
  s.add(POLL_B, 2, 0, 0);
  s.add(280 + 5 * 45, 5, 0, 0);
  return s;
}
constexpr int cap63(int v) { return v < 63 ? v : 63; }
constexpr Sched<44> S4 = make_s4();
constexpr Sched<76> S5 = make_s5();

__global__ __launch_bounds__(256, 1) void k3_kernel(const u16* __restrict__ x, char* __restrict__ ws, u16* __restrict__ out) {
  __shared__ __attribute__((aligned(16))) char smem[160 * 1024];
  const int tid = threadIdx.x, lane = tid & 63, w = wave_id(), r = lane & 31, h = lane >> 5;
  const int bid = blockIdx.x, xcd = bid & 7, idx = bid >> 3;
  const int rb = 4 * (xcd & 3) + (idx & 3), t = 8 * (xcd >> 2) + (idx >> 2);
  const int mb = 2 * rb + (xcd >> 2), nb = idx >> 2;
  const u32 lds0 = lds_addr(smem), voff = lane * 16;
  const char* w1g = ws + W1_OFF + (size_t)t * (280 * 1024);
  const char* w2g = ws + W2_OFF + (size_t)nb * (280 * 1024);
  auto chunk_part = [&](auto C, auto P) {
    constexpr int c = decltype(C)::v, p = decltype(P)::v, o = p == 0 ? 0 : (p == 1 ? 4096 : 7168);
    const char* g = (c < 7 ? w1g + c * 40960 : w2g + (c - 7) * 40960) + w * 10240 + o;
    dma<(p == 0 ? 4 : 3)>(voff, g, lds0 + (c & 3) * 40960 + w * 10240 + o);
  };
  STAMP(ws, 3, 0);
  {
    const int wg = 16 * rb + t, m = 4 * wg + w;
    const char* X2 = ws + X2_OFF;
#pragma unroll
    for (int n = 0; n < 3; ++n) {
      const int pc = w + 4 * n;
      const u32 vx = (u32)(pc * 16 + (lane >> 2)) * 16384u + (u32)(wg * 64 + (lane & 3) * 16);
      const u32 ve = (u32)X2_SZ + (pc == 10 ? (u32)min(lane, 39) * 16u : 0u);
      dma<1>(pc < 10 ? vx : ve, X2, lds0 + R_X2 + pc * 1024);
    }
    u32x2 xr[6];
    {
      const char* src = (const char*)x + (size_t)m * 2560;
      const u32 vo = 40 * r + 8 * h, v2 = 40 * r + 32;
      asm volatile("global_load_dwordx2 %0, %6, %8\n\tglobal_load_dwordx2 %1, %6, %8 offset:16\n\tglobal_load_dwordx2 %2, %7, %8\n\t"
                   "global_load_dwordx2 %3, %6, %8 offset:1280\n\tglobal_load_dwordx2 %4, %6, %8 offset:1296\n\t"
                   "global_load_dwordx2 %5, %7, %8 offset:1280"
                   : "=&v"(xr[0]), "=&v"(xr[1]), "=&v"(xr[2]), "=&v"(xr[3]), "=&v"(xr[4]), "=&v"(xr[5])
                   : "v"(vo), "v"(v2), "s"(src) : "memory");
    }
    u32x4 BR[3], BI[3], Cp[2], Sp[2], Np[2];
    f32x2 tc[8], ts[8];
    {
      float c[16], s[16];
      dft32_consts(c, s, r, h);
      pack_csn(c, s, Cp, Sp, Np);
#pragma unroll
      for (int i = 0; i < 2; ++i) { pin(Cp[i]); pin(Sp[i]); pin(Np[i]); }
      const float rf = (float)r * (1.f / 640.f), r4 = rf * (float)(4 * h);
      static_for<0, 4>([&](auto Q) {
        constexpr int q = decltype(Q)::v;
#pragma unroll
        for (int i = 4 * q; i < 4 * q + 4; ++i) {
          const float rev = rf * (float)crow(i, 0) + r4;
          tc[i >> 1][i & 1] = cosr(rev); ts[i >> 1][i & 1] = sinr(rev);
          if (i < 12) {
            const float rev2 = rev * 32.f, c2 = cosr(rev2), s2 = sinr(rev2);
            const u32 br = pk_f16(c2, -s2), bi = pk_f16(s2, c2);
            BR[q][i & 3] = (i >= 8 && h) ? 0u : br; BI[q][i & 3] = (i >= 8 && h) ? 0u : bi;
          }
        }
        if constexpr (q < 3) { pin(BR[q]); pin(BI[q]); }
        pin(tc[2 * q]); pin(ts[2 * q]); pin(tc[2 * q + 1]); pin(ts[2 * q + 1]);
      });
    }
    wait_vm<6>();
    raw_bar();
    STAMP(ws, 3, 1);
    u32x4 a[3];
    u32 ec[3];
#pragma unroll
    for (int ks = 0; ks < 3; ++ks) {
      const int c = (ks == 2) ? 4 : 2 * ks + h;
      a[ks] = *(const u32x4*)(smem + R_X2 + (r * 5 + c) * 64 + w * 16);
      ec[ks] = *(const u32*)(smem + R_EC + (r * 5 + c) * 4);
    }
    int ex[12];
    u32 em = 0;
#pragma unroll
    for (int i = 0; i < 12; ++i) { ex[i] = (ec[i >> 2] >> (8 * (i & 3))) & 0xff; em = umax(em, (u32)ex[i]); }
    const int E = (int)max64(em);
#pragma unroll
    for (int i = 0; i < 12; ++i) a[i >> 2][i & 3] = pkmul_h(a[i >> 2][i & 3], pow2_h2(ex[i] - (E + 5)));
    const float fs = __builtin_amdgcn_ldexpf(1.f / 1280.f, E - 127 - 9);
    f32x16 Tr = zero16(), Ti = zero16();
#pragma unroll
    for (int ks = 0; ks < 3; ++ks) { Tr = mf16(a[ks], BR[ks], Tr); Ti = mf16(a[ks], BI[ks], Ti); }
    pin(Tr); pin(Ti);
    chunk_part(IC<0>{}, IC<0>{});
    u32x4 P[4];
#pragma unroll
    for (int k = 0; k < 8; ++k) {
      const f32x2 dr = lo2(Tr, 2 * k), di = lo2(Ti, 2 * k);
      P[k >> 2][k & 3] = pk_f16v(dr * tc[k] - di * ts[k]);
      P[2 + (k >> 2)][k & 3] = pk_f16v(di * tc[k] + dr * ts[k]);
    }
    pin(P[0]); pin(P[1]); pin(P[2]); pin(P[3]);
    chunk_part(IC<0>{}, IC<1>{});
    f32x16 Vr = zero16(), Vi = zero16();
#pragma unroll
    for (int s = 0; s < 2; ++s) {
      Vr = mf16(P[s], Cp[s], Vr); Vi = mf16(P[s], Sp[s], Vi);
      Vr = mf16(P[2 + s], Np[s], Vr); Vi = mf16(P[2 + s], Cp[s], Vi);
    }
    pin(Vr); pin(Vi);
    chunk_part(IC<0>{}, IC<2>{});
    asm volatile("s_waitcnt vmcnt(10)" : "+v"(xr[0]), "+v"(xr[1]), "+v"(xr[2]), "+v"(xr[3]), "+v"(xr[4]), "+v"(xr[5])::"memory");
    STAMP(ws, 3, 2);
    f32x2 hv[2][6];
    u32 hp[2][6];
    float mean[2], var[2];
#pragma unroll
    for (int rr = 0; rr < 2; ++rr) {
      f32x2 sm = {0.f, 0.f};
#pragma unroll
      for (int k = 0; k < 6; ++k) {
        const f32x2 y = (rr ? lo2(Vi, 2 * k) : lo2(Vr, 2 * k)) * fs;
        const u32 yb = pk_bf16(y[0], y[1]);
        const u32 xx = xr[3 * rr + (k >> 1)][k & 1];
        const f32x2 hs = f32x2{bf_lo(xx), bf_hi(xx)} + f32x2{bf_lo(yb), bf_hi(yb)};
        u32 hh = pk_bf16(hs[0], hs[1]);
        if (k >= 4) hh = h ? 0u : hh;
        hp[rr][k] = hh;
        hv[rr][k] = f32x2{bf_lo(hh), bf_hi(hh)};
        sm += hv[rr][k];
      }
      mean[rr] = sm[0] + sm[1];
    }
    pin(mean[0]); pin(mean[1]);
    chunk_part(IC<1>{}, IC<0>{});
#pragma unroll
    for (int rr = 0; rr < 2; ++rr) {
      char* hl = smem + R_H + (2 * w + rr) * 1280 + 40 * r;
      *(u32x2*)(hl + 8 * h) = u32x2{hp[rr][0], hp[rr][1]};
      *(u32x2*)(hl + 8 * h + 16) = u32x2{hp[rr][2], hp[rr][3]};
      if (h == 0) *(u32x2*)(hl + 32) = u32x2{hp[rr][4], hp[rr][5]};
    }
#pragma unroll
    for (int rr = 0; rr < 2; ++rr) mean[rr] = sum64(mean[rr]) * (1.f / 640.f);
    pin(mean[0]); pin(mean[1]);
    chunk_part(IC<1>{}, IC<1>{});
#pragma unroll
    for (int rr = 0; rr < 2; ++rr) {
      f32x2 q2 = {0.f, 0.f}, q3 = {0.f, 0.f};
#pragma unroll
      for (int k = 0; k < 4; ++k) { const f32x2 d = hv[rr][k] - mean[rr]; q2 += d * d; }
#pragma unroll
      for (int k = 4; k < 6; ++k) { const f32x2 d = hv[rr][k] - mean[rr]; q3 += d * d; }
      var[rr] = q2[0] + q2[1] + (h ? 0.f : q3[0] + q3[1]);
    }
    pin(var[0]); pin(var[1]);
    chunk_part(IC<1>{}, IC<2>{});
#pragma unroll
    for (int rr = 0; rr < 2; ++rr) var[rr] = sum64(var[rr]) * (1.f / 640.f);
#pragma unroll
    for (int rr = 0; rr < 2; ++rr) {
      float mbn, inv;
      ln_scalars(mean[rr], var[rr], mbn, inv);
      u32 o[6];
#pragma unroll
      for (int k = 0; k < 6; ++k) {
        const f32x2 d0 = hv[rr][k] - mbn;
        const u32 d = pk_bf16(d0[0], d0[1]);
        const f32x2 n0 = f32x2{bf_lo(d), bf_hi(d)} * inv;
        o[k] = pk_bf16(n0[0], n0[1]);
      }
      const int rowl = 2 * w + rr;
      u32x4 o4 = {o[0], o[1], o[2], o[3]};
      *(u32x4*)(smem + R_ST + (h * 32 + r) * 144 + rowl * 16) = o4;
      if (h == 0) { u32x2 o2 = {o[4], o[5]}; *(u32x2*)(smem + R_ST + (64 + (r >> 1)) * 144 + rowl * 16 + (r & 1) * 8) = o2; }
    }
    wait_lgkm0();
    raw_bar();
    STAMP(ws, 3, 3);
    const int row0 = 8 * wg;
    const char* ab = ws + ACT_OFF + (size_t)(row0 >> 5) * (40 * 1024) + (row0 & 31) * 16;
#pragma unroll
    for (int it = 0; it < 3; ++it) {
      const int gi = (tid >> 3) + 32 * it;
      if (gi < 80) {
        const int L = gi < 64 ? (gi & 1) * 32 + (gi >> 1) : gi;
        st16_dev(gi * 512 + (tid & 7) * 16, ab, *(const u32x4*)(smem + R_ST + L * 144 + (tid & 7) * 16));
      }
    }
  }
  STAMP(ws, 3, 4);
  auto pre1 = [&] {
    const char* hb = ws + H_OFF + (size_t)(16 * rb + t) * 10240;
#pragma unroll
    for (int i = 0; i < 4; ++i) {
      const int c = tid - 64 + 192 * i;
      if (c < 640) st16_dev(c * 16, hb, *(const u32x4*)(smem + R_H + c * 16));
    }
#pragma unroll
    for (int g = 0; g < 4; ++g)
      if (3 * g + w - 1 < 10) dma<4>(voff, w1g + 81920 + (3 * g + w - 1) * 4096, lds0 + 81920 + (3 * g + w - 1) * 4096);
  };
  publish_and_wait((u32*)(ws + CNT_OFF + rb * 256), tid, w, pre1);
  STAMP(ws, 3, 5);

  u32 lb[3];
#pragma unroll
  for (int i = 0; i < 3; ++i) { lb[i] = lane * 16 + i * 65536; pin(lb[i]); }
  u32x4 wf[NWF];
  u32 pollv;
  u32* cntA = (u32*)(ws + CNT_OFF + (16 + rb) * 256);
  u32* cntB = (u32*)(ws + CNT_OFF + (32 + rb) * 256);
  auto rdw = [&](auto N) {
    constexpr int n = decltype(N)::v, o = (n % 160) * 1024;
    if constexpr (n < 560) wf[n % NWF] = *(const u32x4*)(smem + lb[o >> 16] + (o & 65535));
  };
  auto boundary = [&](auto WN) {
    wait_lgkm0();
    wait_vm<decltype(WN)::v>();
    raw_bar();
  };
  {
    u32x4 act[40];
    const char* actg = ws + ACT_OFF + (size_t)(4 * rb + w) * (40 * 1024);
    f32x16 acc[7];
    u32x4 gout;
    float ez[2][2];
    const int m = 128 * rb + 32 * w + r;
    const char* gdst = ws + G_OFF + (size_t)((2 * rb + (w >> 1)) * 56) * 4096 + (w & 1) * 2048;
    const u32 gvo = (r >> 4) * 1024 + (r & 15) * 16 + h * 256;
    float k1 = -2.3022081983f;
    pin(k1);
    auto epi = [&](auto U) {
      constexpr int u = decltype(U)::v, z = u & 1, st = (u >> 1) & 7, pr = 2 * (u >> 4) + z, ft = pr / 4, p = pr % 4;
#pragma unroll
      for (int e = 0; e < 2; ++e) {
        const float g = acc[ft][2 * p + e];
        if constexpr (st == 0) ez[z][e] = vmul(g, g);
        if constexpr (st == 1) ez[z][e] = vpoly(ez[z][e], k1);
        if constexpr (st == 2) ez[z][e] = vmul(ez[z][e], g);
        if constexpr (st == 3) ez[z][e] = vexp(ez[z][e]);
        if constexpr (st == 4) { ez[z][e] = vadd1(ez[z][e]); }
        if constexpr (st == 5) ez[z][e] = vrcp(ez[z][e]);
        if constexpr (st == 6) ez[z][e] = vmul(g, ez[z][e]);
        if constexpr (st == 7) ez[z][e] = vmul(ez[z][e], acc[ft][8 + 2 * p + e]);
      }
      if constexpr (st == 7) gout[p] = pk_bf16(ez[z][0], ez[z][1]);
    };
    auto gstore = [&](int ft) {
      const int kk = ft < 4 ? 2 * t + (ft >> 1) : (ft < 6 ? 32 + t : 48 + (t >> 1)), g2 = ft < 6 ? ft & 1 : t & 1;
      st16_dev(gvo, gdst + kk * 4096 + g2 * 512, gout);
    };
    auto events = [&](auto N) {
      static_for<S4.first(decltype(N)::v), S4.first(decltype(N)::v + 1)>([&](auto I) {
        constexpr Ev e = S4.ev[decltype(I)::v];
        {
          if constexpr (e.kind == 0) ld4_dev(act[4 * e.a], act[4 * e.a + 1], act[4 * e.a + 2], act[4 * e.a + 3], voff, actg + e.a * 4096);
          if constexpr (e.kind == 1) chunk_part(IC<e.a>{}, IC<e.b>{});
          if constexpr (e.kind == 3) gstore(e.a);
          if constexpr (e.kind == 2) poll_issue(pollv, cntA);
        }
      });
    };
    events(IC<-1>{});
    static_for<0, PDN>(rdw);
    static_for<0, 280>([&](auto N) {
      constexpr int n = decltype(N)::v;
      constexpr int ft = n < 120 ? n % 3 : n / 40, k16 = n < 120 ? n / 3 : n % 40;
      if constexpr (n < 120 && n % 6 == 0)
        wait_a2<S4.before(n) - (S4.end_of(0, k16 / 4, 0) - 2 + k16 % 4)>(act[k16], act[k16 + 1]);
      mfma32<k16 == 0>(acc[ft], wf[n % NWF], act[k16]);
      __builtin_amdgcn_sched_barrier(0);
      if constexpr ((n + PDN) % 40 == 0 && n + PDN < 280) {
        constexpr int c = (n + PDN) / 40;
        boundary(IC<cap63(c < 2 ? 63 : c == 2 ? S4.before(n) : S4.before(n) - S4.end_of(1, c, 2))>{});
      }
      if constexpr (n == SIG_A) {
        wait_vm<S4.before(n) - S4.end_of(3, 3, 0)>();
        raw_bar();
        signal(cntA, tid);
      }
      if constexpr (n + PDN < 280) rdw(IC<n + PDN>{});
      static_for<0, 2>([&](auto K) {
        constexpr int u = epi_first(n) + decltype(K)::v;
        if constexpr (u < NEPI) { if constexpr (epi_slot(u) == n) epi(IC<u>{}); }
      });
      events(N);
      __builtin_amdgcn_sched_barrier(0);
    });
    STAMP(ws, 3, 9);
    asm volatile("s_nop 7\n\ts_nop 7" : "+v"(acc[6]));
#pragma unroll
    for (int p = 0; p < 4; ++p) {
      const f32x2 g = lo2(acc[6], 2 * p), u = lo2(acc[6], 8 + 2 * p);
      const f32x2 z = g * (g * g * -0.1029432397f + -2.3022081983f);
      const f32x2 den = f32x2{__builtin_amdgcn_exp2f(z[0]), __builtin_amdgcn_exp2f(z[1])} + 1.f;
      const f32x2 ov = (g * f32x2{__builtin_amdgcn_rcpf(den[0]), __builtin_amdgcn_rcpf(den[1])}) * u;
      gout[p] = pk_bf16(ov[0], ov[1]);
    }
    gstore(6);
  }
  STAMP(ws, 3, 10);
    poll_check<S4.before(1000) + 1 - S4.end_of(2, 0, 0)>(pollv, cntA);
  boundary(IC<S4.before(1000) + 1 - S4.end_of(1, 7, 2)>{});
  STAMP(ws, 3, 11);
  {
    u32x4 gf[56];
    u32x2 hv[5];
    const int mrow = 64 * mb + 16 * w + (lane & 15), ncol = 80 * nb + 4 * (lane >> 4);
    const char* gg = ws + G_OFF + (size_t)(mb * 56) * 4096 + w * 1024;
    auto events = [&](auto N) {
      static_for<S5.first(decltype(N)::v), S5.first(decltype(N)::v + 1)>([&](auto I) {
        constexpr Ev e = S5.ev[decltype(I)::v];
        {
          if constexpr (e.kind == 4) ld1_dev(gf[e.a], voff, gg + e.a * 4096);
          if constexpr (e.kind == 1) chunk_part(IC<e.a>{}, IC<e.b>{});
          if constexpr (e.kind == 2) poll_issue(pollv, cntB);
          if constexpr (e.kind == 5) ldh_dev(hv, (u32)(mrow * 640 + ncol) * 2u, ws + H_OFF);
        }
      });
    };
    events(IC<-1>{});
    f32x4 acc[5];
    static_for<280, 280 + PDN>(rdw);
    static_for<280, 560>([&](auto N) {
      constexpr int n = decltype(N)::v, k = (n - 280) / 5, nt = (n - 280) % 5;
      if constexpr (nt == 0 && k % 2 == 0) wait_a2<S5.before(n) - S5.end_of(4, k + 1, 0)>(gf[k], gf[k + 1]);
      mfma16<k == 0>(acc[nt], wf[n % NWF], gf[k]);
      __builtin_amdgcn_sched_barrier(0);
      if constexpr ((n + PDN) % 40 == 0 && n + PDN < 560) {
        constexpr int c = (n + PDN) / 40;
        boundary(IC<cap63(c < 10 ? 63 : S5.before(n) - S5.end_of(1, c, 2))>{});
      }
      if constexpr (n == SIG_B) {
        raw_bar();
        signal(cntB, tid);
      }
      if constexpr (n == CHECK_B) poll_check<S5.before(n) - S5.end_of(2, 0, 0)>(pollv, cntB);
      rdw(IC<n + PDN>{});
      events(N);
      __builtin_amdgcn_sched_barrier(0);
    });
    STAMP(ws, 3, 12);
    asm volatile("s_waitcnt vmcnt(0)\n\ts_nop 7\n\ts_nop 7"
                 : "+v"(hv[0]), "+v"(hv[1]), "+v"(hv[2]), "+v"(hv[3]), "+v"(hv[4]), "+v"(acc[0]), "+v"(acc[1]), "+v"(acc[2]),
                   "+v"(acc[3]), "+v"(acc[4])::"memory");
    u16* op = out + (size_t)mrow * 640 + ncol;
#pragma unroll
    for (int j = 0; j < 5; ++j) {
      const u32 o0 = pk_bf16(acc[j][0], acc[j][1]), o1 = pk_bf16(acc[j][2], acc[j][3]);
      u32x2 o = {pk_bf16(bf_lo(hv[j][0]) + bf_lo(o0), bf_hi(hv[j][0]) + bf_hi(o0)),
                 pk_bf16(bf_lo(hv[j][1]) + bf_lo(o1), bf_hi(hv[j][1]) + bf_hi(o1))};
      *(u32x2*)(op + 16 * j) = o;
    }
  }
  STAMP(ws, 3, 13);
  STAMPE(ws, 3);
}

extern "C" void kernel_launch(void* const* d_in, const int* in_sizes, int n_in, void* d_out, int out_size, void* d_ws,
                              size_t ws_size, hipStream_t stream) {
  const u16* x = (const u16*)d_in[0];
  const u16* wr = (const u16*)d_in[1];
  const u16* wi = (const u16*)d_in[2];
  const u16* wg = (const u16*)d_in[4];
  const u16* wu = (const u16*)d_in[5];
  const u16* wd = (const u16*)d_in[6];
  char* ws = (char*)d_ws;
  k1_kernel<<<K1_GRID + K1_WARM, 256, 0, stream>>>(x, wr, wi, ws);
  k2_kernel<<<K2_GRID, 256, 0, stream>>>(wg, wu, wd, ws);
  k3_kernel<<<256, 256, 0, stream>>>(x, ws, (u16*)d_out);
}
```

```cpp
#include <hip/hip_runtime.h>
#include <hip/hip_bf16.h>
#include <stdint.h>

#define DI __device__ __forceinline__
typedef unsigned short u16;
typedef uint32_t u32;
typedef _Float16 f16;
typedef f16 f16x2 __attribute__((ext_vector_type(2)));
typedef f16 f16x4 __attribute__((ext_vector_type(4)));
typedef f16 f16x8 __attribute__((ext_vector_type(8)));
typedef __bf16 bf16x2 __attribute__((ext_vector_type(2)));
typedef __bf16 bf16x8 __attribute__((ext_vector_type(8)));
typedef short s16x4 __attribute__((ext_vector_type(4)));
typedef float f32x2 __attribute__((ext_vector_type(2)));
typedef float f32x4 __attribute__((ext_vector_type(4)));
typedef float f32x16 __attribute__((ext_vector_type(16)));
typedef u32 u32x2 __attribute__((ext_vector_type(2)));
typedef u32 u32x4 __attribute__((ext_vector_type(4)));
typedef int i32x4 __attribute__((ext_vector_type(4)));
#define LAS __attribute__((address_space(3)))

constexpr int NCOLWG = 162, NSLOT = 4 * NCOLWG;
constexpr size_t X1_OFF = 0;
constexpr size_t X1_SZ = 256ull * NSLOT * 16;
constexpr size_t WT_OFF = X1_OFF + X1_SZ;
constexpr size_t WT_SZ = 323ull * 8192;
constexpr size_t X2_OFF = WT_OFF + WT_SZ;
constexpr size_t X2_SZ = 160ull * 16384;
constexpr size_t EC_OFF = X2_OFF + X2_SZ;
constexpr size_t EC_SZ = 4096;
constexpr size_t H_OFF = EC_OFF + EC_SZ;
constexpr size_t H_SZ = 2048ull * 640 * 2;
constexpr size_t ACT_OFF = H_OFF + H_SZ;
constexpr size_t ACT_SZ = 64ull * 40 * 1024;
constexpr size_t W1_OFF = ACT_OFF + ACT_SZ;
constexpr size_t W1_SZ = 16ull * 280 * 1024;
constexpr size_t W2_OFF = W1_OFF + W1_SZ;
constexpr size_t W2_SZ = 8ull * 56 * 5 * 1024;
constexpr size_t G_OFF = W2_OFF + W2_SZ;
constexpr size_t G_SZ = 32ull * 56 * 4 * 1024;
constexpr size_t CNT_OFF = G_OFF + G_SZ;

DI float asf(u32 u) { return __builtin_bit_cast(float, u); }
DI u32 asu(float f) { return __builtin_bit_cast(u32, f); }
DI u32 pk_bf16(float a, float b) { f32x2 v = {a, b}; return __builtin_bit_cast(u32, __builtin_convertvector(v, bf16x2)); }
DI u32 pk_f16(float a, float b) { f32x2 v = {a, b}; return __builtin_bit_cast(u32, __builtin_convertvector(v, f16x2)); }
DI u32 pk_f16v(f32x2 v) { return __builtin_bit_cast(u32, __builtin_convertvector(v, f16x2)); }
DI float bf_lo(u32 u) { return asf(u << 16); }
DI float bf_hi(u32 u) { return asf(u & 0xffff0000u); }
DI float bfr(float a) { return bf_lo(pk_bf16(a, a)); }
DI float cosr(float rev) { return __builtin_amdgcn_cosf(rev); }
DI float sinr(float rev) { return __builtin_amdgcn_sinf(rev); }
constexpr __host__ __device__ int crow(int reg, int h) { return (reg & 3) + 8 * (reg >> 2) + 4 * h; }
DI u32 umax(u32 a, u32 b) { return a > b ? a : b; }

template <int CTRL> DI u32 dppu(u32 v) { return (u32)__builtin_amdgcn_update_dpp(0, (int)v, CTRL, 0xf, 0xf, true); }
template <int CTRL> DI float dpp(float v) { return asf(dppu<CTRL>(asu(v))); }
DI float sum32(float v) {
  v += dpp<0xB1>(v); v += dpp<0x4E>(v); v += dpp<0x141>(v); v += dpp<0x140>(v);
  auto r = __builtin_amdgcn_permlane16_swap(asu(v), asu(v), false, false);
  return asf(r[0]) + asf(r[1]);
}
DI float sum64(float v) {
  v = sum32(v);
  auto r = __builtin_amdgcn_permlane32_swap(asu(v), asu(v), false, false);
  return asf(r[0]) + asf(r[1]);
}
DI u32 max64(u32 v) {
  v = umax(v, dppu<0xB1>(v)); v = umax(v, dppu<0x4E>(v)); v = umax(v, dppu<0x141>(v)); v = umax(v, dppu<0x140>(v));
  auto r = __builtin_amdgcn_permlane16_swap(v, v, false, false);
  v = umax(r[0], r[1]);
  auto s = __builtin_amdgcn_permlane32_swap(v, v, false, false);
  return umax(s[0], s[1]);
}
DI f16x8 asf16x8(u32x4 v) { return __builtin_bit_cast(f16x8, v); }
DI f32x16 mf16(u32x4 a, u32x4 b, f32x16 c) { return __builtin_amdgcn_mfma_f32_32x32x16_f16(asf16x8(a), asf16x8(b), c, 0, 0, 0); }
DI u32x2 tr_read(const char* p) {
  s16x4 r = __builtin_amdgcn_ds_read_tr16_b64_v4i16((LAS s16x4*)(p));
  return __builtin_bit_cast(u32x2, r);
}
DI f32x16 zero16() { f32x16 z; for (int i = 0; i < 16; ++i) z[i] = 0.f; return z; }
DI int wave_id() { return __builtin_amdgcn_readfirstlane(threadIdx.x >> 6); }
DI f32x2 lo2(const f32x16& v, int i) { return f32x2{v[i], v[i + 1]}; }
DI f32x2 pr2(const f32x4& v, int i) { return f32x2{v[i], v[i + 1]}; }
DI u32 pkmul_h(u32 a, u32 b) {
  const f16x2 r = __builtin_bit_cast(f16x2, a) * __builtin_bit_cast(f16x2, b);
  return __builtin_bit_cast(u32, r);
}
DI u32 pow2_h2(int d) {
  const float s = __builtin_amdgcn_ldexpf(1.f, d);
  return pk_f16(s, s);
}
template <class T> DI void pin(T& v) { asm volatile("" : "+v"(v)); }

DI void ln_scalars(float mean, float var, float& mb, float& inv) {
  mb = bfr(mean);
  float vb = bfr(var);
  float t = bfr(vb + 1e-5f);
  float den = bfr(__builtin_amdgcn_sqrtf(t));
  inv = __builtin_amdgcn_rcpf(den);
}

template <int N> struct IC { static constexpr int v = N; };
template <int I, int N, class F> DI void static_for(F&& f) {
  if constexpr (I < N) { f(IC<I>{}); static_for<I + 1, N>(f); }
}
template <int N> DI void wait_vm() { asm volatile("s_waitcnt vmcnt(%0)" ::"n"(N) : "memory"); }
DI void wait_lgkm0() { asm volatile("s_waitcnt lgkmcnt(0)" ::: "memory"); }
DI void raw_bar() { asm volatile("s_barrier" ::: "memory"); }
DI u32 lds_addr(const char* p) { return (u32)(size_t)(LAS const char*)p; }

template <int N> DI void dma(u32 voff, const void* g, u32 lds) {
  if constexpr (N == 4)
    asm volatile("s_mov_b32 m0, %2\n\ts_nop 0\n\tglobal_load_lds_dwordx4 %0, %1\n\t"
                 "global_load_lds_dwordx4 %0, %1 offset:1024\n\tglobal_load_lds_dwordx4 %0, %1 offset:2048\n\t"
                 "global_load_lds_dwordx4 %0, %1 offset:3072" ::"v"(voff), "s"(g), "s"(lds) : "memory");
  else if constexpr (N == 3)
    asm volatile("s_mov_b32 m0, %2\n\ts_nop 0\n\tglobal_load_lds_dwordx4 %0, %1\n\t"
                 "global_load_lds_dwordx4 %0, %1 offset:1024\n\tglobal_load_lds_dwordx4 %0, %1 offset:2048" ::"v"(voff),
                 "s"(g), "s"(lds) : "memory");
  else if constexpr (N == 2)
    asm volatile("s_mov_b32 m0, %2\n\ts_nop 0\n\tglobal_load_lds_dwordx4 %0, %1\n\t"
                 "global_load_lds_dwordx4 %0, %1 offset:1024" ::"v"(voff), "s"(g), "s"(lds) : "memory");
  else
    asm volatile("s_mov_b32 m0, %2\n\ts_nop 0\n\tglobal_load_lds_dwordx4 %0, %1" ::"v"(voff), "s"(g), "s"(lds) : "memory");
}
template <int N> DI void dma_dev(u32 voff, const void* g, u32 lds) {
  if constexpr (N == 4)
    asm volatile("s_mov_b32 m0, %2\n\ts_nop 0\n\tglobal_load_lds_dwordx4 %0, %1 sc1\n\t"
                 "global_load_lds_dwordx4 %0, %1 offset:1024 sc1\n\tglobal_load_lds_dwordx4 %0, %1 offset:2048 sc1\n\t"
                 "global_load_lds_dwordx4 %0, %1 offset:3072 sc1" ::"v"(voff), "s"(g), "s"(lds) : "memory");
  else if constexpr (N == 3)
    asm volatile("s_mov_b32 m0, %2\n\ts_nop 0\n\tglobal_load_lds_dwordx4 %0, %1 sc1\n\t"
                 "global_load_lds_dwordx4 %0, %1 offset:1024 sc1\n\tglobal_load_lds_dwordx4 %0, %1 offset:2048 sc1" ::"v"(voff),
                 "s"(g), "s"(lds) : "memory");
  else if constexpr (N == 2)
    asm volatile("s_mov_b32 m0, %2\n\ts_nop 0\n\tglobal_load_lds_dwordx4 %0, %1 sc1\n\t"
                 "global_load_lds_dwordx4 %0, %1 offset:1024 sc1" ::"v"(voff), "s"(g), "s"(lds) : "memory");
  else
    asm volatile("s_mov_b32 m0, %2\n\ts_nop 0\n\tglobal_load_lds_dwordx4 %0, %1 sc1" ::"v"(voff), "s"(g), "s"(lds) : "memory");
}
DI void st16_dev(u32 voff, const void* g, u32x4 v) {
  asm volatile("global_store_dwordx4 %0, %1, %2 sc1\n\ts_nop 1" ::"v"(voff), "v"(v), "s"(g) : "memory");
}
DI void st16_wt(void* p, u32x4 v) {
  asm volatile("global_store_dwordx4 %0, %1, off sc1\n\ts_nop 1" ::"v"(p), "v"(v) : "memory");
}
DI void ld4_dev(u32x4& a0, u32x4& a1, u32x4& a2, u32x4& a3, u32 voff, const void* g) {
  asm volatile("global_load_dwordx4 %0, %4, %5 sc1\n\tglobal_load_dwordx4 %1, %4, %5 offset:1024 sc1\n\t"
               "global_load_dwordx4 %2, %4, %5 offset:2048 sc1\n\tglobal_load_dwordx4 %3, %4, %5 offset:3072 sc1"
               : "=&a"(a0), "=&a"(a1), "=&a"(a2), "=&a"(a3) : "v"(voff), "s"(g) : "memory");
}
DI void ld1_dev(u32x4& a0, u32 voff, const void* g) {
  asm volatile("global_load_dwordx4 %0, %1, %2 sc1" : "=&a"(a0) : "v"(voff), "s"(g) : "memory");
}
DI void ldh_dev(u32x2 (&hv)[5], u32 vo, const void* g) {
  asm volatile("global_load_dwordx2 %0, %5, %6 sc1\n\tglobal_load_dwordx2 %1, %5, %6 offset:32 sc1\n\t"
               "global_load_dwordx2 %2, %5, %6 offset:64 sc1\n\tglobal_load_dwordx2 %3, %5, %6 offset:96 sc1\n\t"
               "global_load_dwordx2 %4, %5, %6 offset:128 sc1"
               : "=&v"(hv[0]), "=&v"(hv[1]), "=&v"(hv[2]), "=&v"(hv[3]), "=&v"(hv[4]) : "v"(vo), "s"(g) : "memory");
}
template <int N> DI void wait_a2(u32x4& a0, u32x4& a1) {
  asm volatile("s_waitcnt vmcnt(%2)" : "+a"(a0), "+a"(a1) : "n"(N) : "memory");
}
DI float vmul(float a, float b) { float d; asm volatile("v_mul_f32 %0, %1, %2" : "=v"(d) : "v"(a), "v"(b)); return d; }
DI float vpoly(float z, float k1) { float d; asm volatile("v_fmamk_f32 %0, %1, 0xbdd2d3e8, %2" : "=v"(d) : "v"(z), "v"(k1)); return d; }
DI float vadd1(float a) { float d; asm volatile("v_add_f32 %0, 1.0, %1" : "=v"(d) : "v"(a)); return d; }
DI float vexp(float a) { float d; asm volatile("v_exp_f32 %0, %1" : "=v"(d) : "v"(a)); return d; }
DI float vrcp(float a) { float d; asm volatile("v_rcp_f32 %0, %1" : "=v"(d) : "v"(a)); return d; }
template <bool FIRST> DI void mfma_h(f32x16& d, const u32x4& a, const u32x4& b) {
  if constexpr (FIRST) asm volatile("s_nop 1\n\tv_mfma_f32_32x32x16_f16 %0, %1, %2, 0" : "=&v"(d) : "v"(a), "v"(b));
  else asm volatile("s_nop 1\n\tv_mfma_f32_32x32x16_f16 %0, %1, %2, %0" : "+v"(d) : "v"(a), "v"(b));
}
DI void mfma_done(f32x16& a, f32x16& b) { asm volatile("s_nop 7\n\ts_nop 7" : "+v"(a), "+v"(b)); }
template <bool FIRST> DI void mfma32(f32x16& d, const u32x4& a, const u32x4& b) {
  if constexpr (FIRST) asm volatile("v_mfma_f32_32x32x16_bf16 %0, %1, %2, 0" : "=&v"(d) : "v"(a), "a"(b));
  else asm volatile("v_mfma_f32_32x32x16_bf16 %0, %1, %2, %0" : "+v"(d) : "v"(a), "a"(b));
}
template <bool FIRST> DI void mfma16(f32x4& d, const u32x4& a, const u32x4& b) {
  if constexpr (FIRST) asm volatile("v_mfma_f32_16x16x32_bf16 %0, %1, %2, 0" : "=&v"(d) : "v"(a), "a"(b));
  else asm volatile("v_mfma_f32_16x16x32_bf16 %0, %1, %2, %0" : "+v"(d) : "v"(a), "a"(b));
}
DI void signal(u32* cnt, int tid) {
  if (tid == 0) __hip_atomic_fetch_add(cnt, 1u, __ATOMIC_RELAXED, __HIP_MEMORY_SCOPE_AGENT);
}
DI void poll_issue(u32& v, const u32* cnt) {
  asm volatile("global_load_dword %0, %1, %2 sc1" : "=&v"(v) : "v"(0), "s"(cnt) : "memory");
}
template <int N> DI void poll_check(u32& v, u32* cnt, u32 target = 16u) {
  asm volatile("s_waitcnt vmcnt(%1)" : "+v"(v) : "n"(N) : "memory");
  if (__builtin_amdgcn_readfirstlane(v) < target) {
    for (int it = 0; it < (1 << 16); ++it) {
      if (__builtin_amdgcn_readfirstlane(__hip_atomic_load(cnt, __ATOMIC_RELAXED, __HIP_MEMORY_SCOPE_AGENT)) >= target) break;
      __builtin_amdgcn_s_sleep(1);
    }
  }
  asm volatile("" ::: "memory");
}
template <class F> DI void publish_and_wait(u32* cnt, int tid, int w, F&& idle) {
  asm volatile("s_waitcnt vmcnt(0) lgkmcnt(0)" ::: "memory");
  raw_bar();
  if (w == 0) {
    if (tid == 0) __hip_atomic_fetch_add(cnt, 1u, __ATOMIC_RELAXED, __HIP_MEMORY_SCOPE_AGENT);
    for (int it = 0; it < (1 << 16); ++it) {
      const u32 v = __builtin_amdgcn_readfirstlane(__hip_atomic_load(cnt, __ATOMIC_RELAXED, __HIP_MEMORY_SCOPE_AGENT));
      if (v >= 16u) break;
      __builtin_amdgcn_s_sleep(1);
    }
  } else idle();
  raw_bar();
}

constexpr double PI_ = 3.14159265358979323846;
constexpr double ccos_(double x) {
  double t = 1, s = 1;
  for (int k = 1; k <= 16; ++k) { t *= -x * x / ((2 * k - 1) * (2 * k)); s += t; }
  return s;
}
constexpr double c2048(int k) { k &= 2047; k = k > 1024 ? 2048 - k : k; return ccos_(2 * PI_ * k / 2048); }
constexpr u32 fb(double x) { return __builtin_bit_cast(u32, (float)x); }

struct ColTask { int a1, b1, am, bm; bool valid; };
constexpr __host__ __device__ ColTask col_task(int g, int p) {
  if (g < 150) { const int a1 = g / 10 + 1, b1 = 2 * (g % 10) + p; return {a1, b1, 32 - a1, 19 - b1, true}; }
  if (g < 155) { const int b1 = 2 * (g - 150) + p; return {16, b1, 16, 19 - b1, true}; }
  if (g == 155) return {0, 0, 0, 0, p == 0};
  if (g == 156) return {0, 10, 0, 10, p == 0};
  const int b1 = 2 * (g - 157) + 1 + p;
  return {0, b1 < 10 ? b1 : 9, 0, b1 < 10 ? 20 - b1 : 11, b1 < 10};
}
struct SlotMap {
  u32 v[768];
  constexpr SlotMap() : v() {
    for (int slot = 0; slot < NSLOT; ++slot) {
      const int mir = (slot >> 1) & 1;
      const ColTask t = col_task(slot >> 2, slot & 1);
      const int q1 = t.a1 + 32 * t.b1, a = mir ? t.am : t.a1, b = mir ? t.bm : t.b1;
      const bool cj = mir ? !(q1 > 320) : (q1 > 320);
      v[slot] = (u32)(33 * b + a) | (cj ? 0x80000000u : 0u);
    }
  }
};
template <int SD> struct ThetaTab {
  u32 v[4 * 256];
  constexpr ThetaTab() : v() {
    for (int l = 0; l < 64; ++l)
      for (int n = 0; n < 8; ++n) {
        const int p = 16 * SD + (l & 15) + 32 * crow(4 * (n >> 1) + 2 * ((l >> 4) & 1) + (n & 1), l >> 5);
        v[((n >> 2) * 64 + l) * 4 + (n & 3)] = fb(c2048(p));
        v[((2 + (n >> 2)) * 64 + l) * 4 + (n & 3)] = fb(c2048(p + 1536));
      }
  }
};
struct Theta { ThetaTab<0> a; ThetaTab<1> b; };
static __device__ const __attribute__((aligned(128))) SlotMap g_smap{};
static __device__ const __attribute__((aligned(128))) Theta g_theta{};

DI void dft32_consts(float (&c)[8], float (&s)[8], int r, int h) {
  const float rf = (float)r * (1.f / 32.f), r4 = rf * (float)(4 * h);
#pragma unroll
  for (int i = 0; i < 8; ++i) { const float rev = rf * (float)crow(i, 0) + r4; c[i] = cosr(rev); s[i] = sinr(rev); }
}
DI void pack_csn(const float (&c)[8], const float (&s)[8], int r, u32x4 (&Cp)[2], u32x4 (&Sp)[2], u32x4 (&Np)[2]) {
  const u32 sm = (r & 1) ? 0x80008000u : 0u;
#pragma unroll
  for (int i = 0; i < 4; ++i) {
    Cp[0][i] = pk_f16(c[2 * i], c[2 * i + 1]); Sp[0][i] = pk_f16(s[2 * i], s[2 * i + 1]);
    Np[0][i] = Sp[0][i] ^ 0x80008000u;
    Cp[1][i] = Cp[0][i] ^ sm; Sp[1][i] = Sp[0][i] ^ sm; Np[1][i] = Np[0][i] ^ sm;
  }
}

constexpr int K1_IMG = 1344;
constexpr int K1_PLANE = 33 * 20 * 4;
constexpr int K1_PL0 = 8 * K1_IMG;
constexpr int K1_LDS = 22528;
constexpr int K1_WT_WGS = 128;
constexpr int K1_GRID = 256 + K1_WT_WGS;
static_assert(K1_PL0 + 4 * K1_PLANE <= K1_LDS, "LDS");

DI void k1_row(const u16* __restrict__ x, char* __restrict__ X1, char* smem, int wg) {
  const int tid = threadIdx.x, lane = tid & 63, w = wave_id(), r = lane & 31, h = lane >> 5;
  const int b2 = wg >> 3, aq = wg & 7;
  const int m = 32 * (4 * aq + w) + b2;
  u32x2 v[5];
  u32 e[3];
  {
    const char* src = (const char*)x + (size_t)m * 2560;
    const u32 vo = h * 1280 + r * 8;
    asm volatile("global_load_dwordx2 %0, %5, %6\n\tglobal_load_dwordx2 %1, %5, %6 offset:256\n\t"
                 "global_load_dwordx2 %2, %5, %6 offset:512\n\tglobal_load_dwordx2 %3, %5, %6 offset:768\n\t"
                 "global_load_dwordx2 %4, %5, %6 offset:1024"
                 : "=&v"(v[0]), "=&v"(v[1]), "=&v"(v[2]), "=&v"(v[3]), "=&v"(v[4]) : "v"(vo), "s"(src) : "memory");
    asm volatile("global_load_dword %0, %3, %4\n\tglobal_load_dword %1, %3, %4 offset:1024\n\t"
                 "global_load_dword %2, %3, %4 offset:2048"
                 : "=&v"(e[0]), "=&v"(e[1]), "=&v"(e[2]) : "v"(tid * 4), "s"(g_smap.v) : "memory");
  }
  u32x4 C1[2], S1[2], N1[2], C2, S2, N2, MR, MI;
  f32x2 tc[6], ts[6];
  {
    float c8[8], s8[8], c[12], s[12];
    dft32_consts(c8, s8, r, h);
    pack_csn(c8, s8, r, C1, S1, N1);
    const float rf = (float)r * (1.f / 640.f), r4 = rf * (float)(4 * h);
#pragma unroll
    for (int i = 0; i < 12; ++i) {
      const float rev = rf * (float)crow(i, 0) + r4;
      tc[i >> 1][i & 1] = cosr(rev); ts[i >> 1][i & 1] = sinr(rev);
      const float rev2 = rev * 32.f;
      c[i] = cosr(rev2); s[i] = sinr(rev2);
    }
#pragma unroll
    for (int i = 0; i < 4; ++i) {
      C2[i] = pk_f16(c[2 * i], c[2 * i + 1]); S2[i] = pk_f16(s[2 * i], s[2 * i + 1]); N2[i] = pk_f16(-s[2 * i], -s[2 * i + 1]);
    }
#pragma unroll
    for (int i = 0; i < 2; ++i) {
      const u32 cc = h ? 0u : pk_f16(c[8 + 2 * i], c[9 + 2 * i]), ss = h ? 0u : pk_f16(s[8 + 2 * i], s[9 + 2 * i]);
      MR[i] = cc; MR[2 + i] = ss; MI[i] = ss ^ 0x80008000u; MI[2 + i] = cc;
    }
#pragma unroll
    for (int i = 0; i < 2; ++i) { pin(C1[i]); pin(S1[i]); pin(N1[i]); }
    pin(C2); pin(S2); pin(N2); pin(MR); pin(MI);
#pragma unroll
    for (int i = 0; i < 6; ++i) { pin(tc[i]); pin(ts[i]); }
  }
  asm volatile("s_waitcnt vmcnt(3)" : "+v"(v[0]), "+v"(v[1]), "+v"(v[2]), "+v"(v[3]), "+v"(v[4])::"memory");
  f32x2 f[10];
#pragma unroll
  for (int i = 0; i < 5; ++i) {
    f[2 * i] = f32x2{bf_lo(v[i][0]), bf_hi(v[i][0])};
    f[2 * i + 1] = f32x2{bf_lo(v[i][1]), bf_hi(v[i][1])};
  }
  f32x2 s2 = f[0];
#pragma unroll
  for (int i = 1; i < 10; ++i) s2 += f[i];
  const float mean = sum32(s2[0] + s2[1]) * (1.f / 640.f);
  f32x2 q2 = {0.f, 0.f};
#pragma unroll
  for (int i = 0; i < 10; ++i) { const f32x2 d = f[i] - mean; q2 += d * d; }
  const float var = sum32(q2[0] + q2[1]) * (1.f / 640.f);
  float mb, inv;
  ln_scalars(mean, var, mb, inv);
  char* img = smem + w * (2 * K1_IMG);
#pragma unroll
  for (int i = 0; i < 5; ++i) {
    u32x2 o;
#pragma unroll
    for (int k = 0; k < 2; ++k) {
      const f32x2 d0 = f[2 * i + k] - mb;
      const u32 d = pk_bf16(d0[0], d0[1]);
      const f32x2 n0 = f32x2{bf_lo(d), bf_hi(d)} * inv;
      const u32 n = pk_bf16(n0[0], n0[1]);
      o[k] = pk_f16(bf_lo(n), bf_hi(n));
    }
    *(u32x2*)(img + h * K1_IMG + 8 * (r + 32 * i)) = o;
  }
  if (r < 8) { u32x2 z = {0u, 0u}; *(u32x2*)(img + h * K1_IMG + 1280 + 8 * r) = z; }
  asm volatile("" ::: "memory");
  const int q4 = (lane & 15) >> 2, p4 = lane & 3, blk = (lane >> 4) & 1;
  const char* tb = img + (4 * h + q4) * 40 + 32 * blk + 8 * p4;
  f32x16 Dr = zero16(), Di = zero16();
#pragma unroll
  for (int s = 0; s < 2; ++s) {
    u32x2 r0 = tr_read(tb + 640 * s), r1 = tr_read(tb + 640 * s + 320);
    u32x2 i0 = tr_read(tb + K1_IMG + 640 * s), i1 = tr_read(tb + K1_IMG + 640 * s + 320);
    u32x4 ar = {r0[0], r0[1], r1[0], r1[1]}, ai = {i0[0], i0[1], i1[0], i1[1]};
    Dr = mf16(ar, C1[s], Dr); Di = mf16(ai, C1[s], Di);
    Dr = mf16(ai, S1[s], Dr); Di = mf16(ar, N1[s], Di);
  }
  u32 er[6], ei[6];
#pragma unroll
  for (int k = 0; k < 6; ++k) {
    const f32x2 dr = lo2(Dr, 2 * k), di = lo2(Di, 2 * k);
    er[k] = pk_f16v(dr * tc[k] + di * ts[k]);
    ei[k] = pk_f16v(di * tc[k] - dr * ts[k]);
  }
  const u32x4 pr = {er[0], er[1], er[2], er[3]}, pi = {ei[0], ei[1], ei[2], ei[3]}, pm = {er[4], er[5], ei[4], ei[5]};
  f32x16 Xr = zero16(), Xi = zero16();
  Xr = mf16(C2, pr, Xr); Xi = mf16(N2, pr, Xi);
  Xr = mf16(S2, pi, Xr); Xi = mf16(C2, pi, Xi);
  Xr = mf16(MR, pm, Xr); Xi = mf16(MI, pm, Xi);
  char* planes = smem + K1_PL0;
  u32* pl = (u32*)(planes + w * K1_PLANE) + 33 * 4 * h + r;
#pragma unroll
  for (int i = 0; i < 8; ++i) pl[33 * crow(i, 0)] = pk_f16(Xr[i], Xi[i]);
  if (h == 0) {
#pragma unroll
    for (int i = 8; i < 12; ++i) pl[33 * crow(i, 0)] = pk_f16(Xr[i], Xi[i]);
  }
  asm volatile("s_waitcnt vmcnt(0) lgkmcnt(0)" : "+v"(e[0]), "+v"(e[1]), "+v"(e[2])::"memory");
  raw_bar();
  u32x4* dst = (u32x4*)(X1 + (size_t)(wg >> 1) * (NSLOT * 32)) + (aq & 1) * 4 + (tid >> 2) * 8 + (tid & 3);
#pragma unroll
  for (int it = 0; it < 3; ++it) {
    if (it < 2 || tid < NSLOT - 512) {
      const u32* sp = (const u32*)planes + (e[it] & 0xffffu);
      const u32 cj = e[it] & 0x80000000u;
      u32x4 o = {sp[0] ^ cj, sp[K1_PLANE / 4] ^ cj, sp[2 * (K1_PLANE / 4)] ^ cj, sp[3 * (K1_PLANE / 4)] ^ cj};
      st16_wt(dst + 512 * it, o);
    }
  }
}

DI int wt_off(int i, int h, int a) { return (((a >> 4) * 4 + (i >> 1)) * 64 + h * 32 + (i & 1) * 16 + (a & 15)) * 16; }

DI void k1_wt(const u16* __restrict__ wr, const u16* __restrict__ wi, char* __restrict__ WT, char* smem, int wg) {
  const int tid = threadIdx.x, lane = tid & 63, w = wave_id();
  const int qh = wg & 1, ao = (wg >> 1) & 3, h = (wg >> 3) & 1, i = wg >> 4;
  const int p0 = 8 * ao + 32 * (crow(2 * i, 0) + 4 * h), q0 = 161 * qh, nq = 161 - qh;
  {
    const char* src = (const char*)(w >> 1 ? wi : wr);
    const u32 lds = lds_addr(smem) + (w >> 1) * 11264;
#pragma unroll
    for (int n = 0; n < 6; ++n) {
      const int ins = (w & 1) * 6 + n;
      if (ins < 11) {
        const int e = ins * 64 + lane, row = e / 22, c = e - 22 * row;
        const int p = p0 + (row & 7) + ((row >> 3) & 1) * 1024 + (row >> 4) * 32;
        const u32 vo = min((u32)((642 * p + 2 * q0) & ~15) + 16u * c, 2048u * 642u - 16u);
        dma<1>(vo, src, lds + ins * 1024);
      }
    }
  }
  wait_vm<0>();
  raw_bar();
  const int alo = tid & 7, a = 8 * ao + alo;
  const char* lb = smem + alo * 352 + 2 * ((alo + q0) & 7) + 2 * (tid >> 3);
  char* ob = WT + (size_t)(q0 + (tid >> 3)) * 8192 + wt_off(i, h, a);
#pragma unroll
  for (int it = 0; it < 6; ++it) {
    const int ql = (tid >> 3) + 32 * it;
    if (ql < nq) {
      u32x4 o;
#pragma unroll
      for (int d = 0; d < 4; ++d)
        o[d] = (u32) * (const u16*)(lb + 64 * it + d * 2816) | ((u32) * (const u16*)(lb + 64 * it + d * 2816 + 11264) << 16);
      st16_wt(ob + (size_t)it * (32 * 8192), o);
      if (it == 0 || it == 4) {
        const int q = q0 + ql;
        if (q == 0 || q == 320) {
          char* mb = WT + (size_t)(q ? 322 : 321) * 8192;
#pragma unroll
          for (int d = 0; d < 4; ++d) {
            const int k = p0 + alo + (d & 1) * 1024 + (d >> 1) * 32, kp = (2048 - k) & 2047;
            const int pp = kp & 1023, half = kp >> 10, a2 = pp & 31, bb = pp >> 5;
            const int e = (bb & 3) + 4 * (bb >> 3);
            *(u32*)(mb + wt_off(e >> 1, (bb >> 2) & 1, a2) + (e & 1) * 8 + half * 4) = o[d];
          }
        }
      }
    }
  }
}

DI void touch_lines(const char* g, int n, u32 limit, u32 lds) {
  for (int i = 0; i < n; ++i)
    asm volatile("s_mov_b32 m0, %2\n\ts_nop 0\n\tglobal_load_lds_dword %0, %1" ::"v"(min((u32)(i * 256 + threadIdx.x) * 128u, limit)),
                 "s"(g), "s"(lds) : "memory");
}
constexpr int K1_WARM = 8;
__global__ __launch_bounds__(256, 2) void k1_kernel(const u16* __restrict__ x, const u16* __restrict__ wr,
                                                    const u16* __restrict__ wi, char* __restrict__ ws) {
  __shared__ __attribute__((aligned(16))) char smem[K1_LDS];
  const int bid = blockIdx.x;
  if (bid < 256) k1_row(x, ws + X1_OFF, smem, (bid & 7) * 32 + (bid >> 3));
  else if (bid >= K1_GRID) {
    {
      const char *c2, *c3;
      asm volatile("s_getpc_b64 s[20:21]\n\ts_add_u32 s20, s20, _Z9k2_kernelPKtS0_Pc@rel32@lo+4\n\t"
                   "s_addc_u32 s21, s21, _Z9k2_kernelPKtS0_Pc@rel32@hi+12\n\ts_mov_b64 %0, s[20:21]\n\t"
                   "s_getpc_b64 s[20:21]\n\ts_add_u32 s20, s20, _Z9k3_kernelPKtS0_PcPt@rel32@lo+4\n\t"
                   "s_addc_u32 s21, s21, _Z9k3_kernelPKtS0_PcPt@rel32@hi+12\n\ts_mov_b64 %1, s[20:21]"
                   : "=s"(c2), "=s"(c3)::"s20", "s21", "scc");
      touch_lines(c2, 1, 10u * 1024u, lds_addr(smem));
      touch_lines(c3, 1, 27u * 1024u, lds_addr(smem));
      touch_lines((const char*)&g_theta, 1, 8u * 1024u - 4u, lds_addr(smem));
    }
  } else {
    if (bid == 256 && threadIdx.x < 52) *(u32*)(ws + CNT_OFF + threadIdx.x * 256) = 0u;
    k1_wt(wr, wi, ws + WT_OFF, smem, bid - 256);
  }
}

constexpr int K2_X = 0, K2_ST = 0, K2_W = 16384, K2_TH = 32768, K2_E1 = 49152, K2_E2 = 57344, K2_EB = 65536;
constexpr int K2_LDS = 66 * 1024;
constexpr int K2_GRID = NCOLWG + 80;
static_assert(K2_EB + 64 <= K2_LDS, "LDS");

DI void k2_col(const char* __restrict__ X1, const char* __restrict__ WT, char* __restrict__ X2, unsigned char* __restrict__ ecol,
               char* smem, int wg) {
  const int tid = threadIdx.x, lane = tid & 63, w = wave_id(), r = lane & 31, h = lane >> 5;
  const int pw = w & 1, sd = w >> 1, rho = (lane >> 4) & 1;
  const u32 lds0 = lds_addr(smem);
  const u32 l16 = lane * 16;
  {
    const char* xb = X1 + (size_t)wg * 128 + (size_t)w * (NSLOT * 32);
#pragma unroll
    for (int n = 0; n < 4; ++n) {
      const int rr = n * 8 + (lane >> 3);
      const u32 vo = (u32)rr * (u32)(4 * NSLOT * 32) + (u32)(((lane & 7) ^ ((rr >> 1) & 7)) * 16);
      dma<1>(vo, xb, lds0 + K2_X + (w * 4 + n) * 1024);
    }
  }
  const ColTask tk = col_task(wg, pw);
  const int a1 = tk.a1, b1 = tk.b1, am = tk.am, bm = tk.bm;
  const bool valid = tk.valid;
  const bool direct = wg >= 155, self = wg == 155 || wg == 156;
  const int q1 = a1 + 32 * b1, qm = am + 32 * bm;
  const bool swp = q1 > 320;
  const int task = (self && pw) ? (wg == 155 ? 321 : 322) : (swp ? qm : q1);
  dma<4>(l16, WT + (size_t)task * 8192 + sd * 4096, lds0 + K2_W + (pw * 2 + sd) * 4096);
  dma<4>(l16, (const char*)&g_theta + sd * 4096, lds0 + K2_TH + w * 4096);
  u32x4 BR[4], BI[4], Cp[2], Sp[2], Np[2];
  f32x2 tc[8], ts[8];
  {
    float c[8], s[8];
    dft32_consts(c, s, r, h);
    pack_csn(c, s, r, Cp, Sp, Np);
    const u32 sm = (r & 1) ? 0x80008000u : 0u;
#pragma unroll
    for (int i = 0; i < 8; ++i) {
      BR[i >> 2][i & 3] = pk_f16(c[i], s[i]); BI[i >> 2][i & 3] = pk_f16(-s[i], c[i]);
      BR[2 + (i >> 2)][i & 3] = BR[i >> 2][i & 3] ^ sm; BI[2 + (i >> 2)][i & 3] = BI[i >> 2][i & 3] ^ sm;
    }
    const float rf = (float)r * (1.f / 1024.f), r4 = rf * (float)(4 * h);
#pragma unroll
    for (int i = 0; i < 16; ++i) {
      const float rev = rf * (float)crow(i, 0) + r4;
      tc[i >> 1][i & 1] = cosr(rev); ts[i >> 1][i & 1] = sinr(rev);
    }
#pragma unroll
    for (int i = 0; i < 4; ++i) { pin(BR[i]); pin(BI[i]); }
#pragma unroll
    for (int i = 0; i < 2; ++i) { pin(Cp[i]); pin(Sp[i]); pin(Np[i]); }
#pragma unroll
    for (int i = 0; i < 8; ++i) { pin(tc[i]); pin(ts[i]); }
  }
  wait_vm<8>();
  raw_bar();
  const int slot = (((sd == 1) != swp) ? 2 : 0) + pw;
  f32x16 Zr = zero16(), Zi = zero16();
  {
    const char* xr = smem + K2_X + r * 128 + (((h * 4 + slot) ^ ((r >> 1) & 7)) * 16);
#pragma unroll
    for (int ks = 0; ks < 4; ++ks) {
      const u32x4 a = *(const u32x4*)(xr + ks * 4096);
      Zr = mf16(a, BR[ks], Zr); Zi = mf16(a, BI[ks], Zi);
    }
  }
  u32x4 P[4];
#pragma unroll
  for (int k = 0; k < 8; ++k) {
    const f32x2 dr = lo2(Zr, 2 * k), di = lo2(Zi, 2 * k);
    P[k >> 2][k & 3] = pk_f16v(dr * tc[k] + di * ts[k]);
    P[2 + (k >> 2)][k & 3] = pk_f16v(di * tc[k] - dr * ts[k]);
  }
  {
    u32x4* e1w = (u32x4*)(smem + K2_E1 + w * 2048) + (h * 16 + (lane & 15));
    const u32x4* e1r = (const u32x4*)(smem + K2_E1 + (w ^ 2) * 2048) + (h * 16 + (lane & 15));
    if (rho != sd) {
#pragma unroll
      for (int f = 0; f < 4; ++f) e1w[32 * f] = P[f];
    }
    wait_lgkm0();
    raw_bar();
    if (rho != sd) {
#pragma unroll
      for (int f = 0; f < 4; ++f) P[f] = e1r[32 * f];
    }
  }
  Zr = zero16(); Zi = zero16();
#pragma unroll
  for (int s = 0; s < 2; ++s) {
    Zr = mf16(Cp[s], P[s], Zr); Zi = mf16(Cp[s], P[2 + s], Zi);
    Zr = mf16(Sp[s], P[2 + s], Zr); Zi = mf16(Np[s], P[s], Zi);
  }
  f32x2 alr[4], ali[4], ber[4], bei[4], dcr[4], dci[4];
  pin(Zr); pin(Zi);
  wait_vm<0>();
  if (self) raw_bar();
  {
    const u32x4* wl = (const u32x4*)(smem + K2_W + (pw * 2 + sd) * 4096) + lane;
    const u32x4* wsl = (const u32x4*)(smem + K2_W + (2 + sd) * 4096) + lane;
    const f32x4* th = (const f32x4*)(smem + K2_TH + w * 4096) + lane;
#pragma unroll
    for (int g = 0; g < 4; ++g) {
      const u32x4 t = wl[64 * g];
      f32x2 w0r = {bf_lo(t[0]), bf_lo(t[2])}, w0i = {bf_hi(t[0]), bf_hi(t[2])};
      f32x2 w1r = {bf_lo(t[1]), bf_lo(t[3])}, w1i = {bf_hi(t[1]), bf_hi(t[3])};
      if (self) {
        const u32x4 u = wsl[64 * g];
        w0r = (w0r + f32x2{bf_lo(u[0]), bf_lo(u[2])}) * 0.5f; w0i = (w0i - f32x2{bf_hi(u[0]), bf_hi(u[2])}) * 0.5f;
        w1r = (w1r + f32x2{bf_lo(u[1]), bf_lo(u[3])}) * 0.5f; w1i = (w1i - f32x2{bf_hi(u[1]), bf_hi(u[3])}) * 0.5f;
      }
      const f32x4 thc = th[64 * (g >> 1)], ths = th[64 * (2 + (g >> 1))];
      const f32x2 ct = pr2(thc, 2 * (g & 1)), st = pr2(ths, 2 * (g & 1));
      const f32x2 sr = w0r + w1r, si = w0i + w1i, dr = w0r - w1r, di = w0i - w1i;
      alr[g] = sr - dr * st; ali[g] = si - di * st; ber[g] = sr + dr * st; bei[g] = si + di * st;
      dcr[g] = dr * ct; dci[g] = di * ct;
    }
  }
  f32x2 nur[4], nui[4], nvr[4], nvi[4];
  float mx = 0.f;
#pragma unroll
  for (int g = 0; g < 4; ++g) {
    f32x2 ur, ui, vr, vi;
#pragma unroll
    for (int k = 0; k < 2; ++k) {
      auto sr_ = __builtin_amdgcn_permlane16_swap(asu(Zr[4 * g + k]), asu(Zr[4 * g + 2 + k]), false, false);
      auto si_ = __builtin_amdgcn_permlane16_swap(asu(Zi[4 * g + k]), asu(Zi[4 * g + 2 + k]), false, false);
      ur[k] = asf(sr_[0]); vr[k] = asf(sr_[1]); ui[k] = asf(si_[0]); vi[k] = asf(si_[1]);
    }
    nur[g] = alr[g] * ur - ali[g] * ui - dcr[g] * vi - dci[g] * vr;
    nui[g] = alr[g] * ui + ali[g] * ur + dcr[g] * vr - dci[g] * vi;
    nvr[g] = ber[g] * vr - bei[g] * vi + dcr[g] * ui + dci[g] * ur;
    nvi[g] = ber[g] * vi + bei[g] * vr - dcr[g] * ur + dci[g] * ui;
#pragma unroll
    for (int k = 0; k < 2; ++k)
      mx = fmaxf(fmaxf(mx, fmaxf(fabsf(nur[g][k]), fabsf(nui[g][k]))), fmaxf(fabsf(nvr[g][k]), fabsf(nvi[g][k])));
  }
  const int eb = (int)umax(max64(asu(mx)) >> 23, 4u);
  const float sc = asf((u32)(258 - eb) << 23);
#pragma unroll
  for (int g = 0; g < 4; ++g) {
    auto qr = __builtin_amdgcn_permlane16_swap(pk_f16v(nur[g] * sc), pk_f16v(nvr[g] * sc), false, false);
    auto qi = __builtin_amdgcn_permlane16_swap(pk_f16v(nui[g] * sc), pk_f16v(nvi[g] * sc), false, false);
    P[g >> 1][2 * (g & 1)] = qr[0]; P[g >> 1][2 * (g & 1) + 1] = qr[1];
    P[2 + (g >> 1)][2 * (g & 1)] = qi[0]; P[2 + (g >> 1)][2 * (g & 1) + 1] = qi[1];
  }
  Zr = zero16(); Zi = zero16();
#pragma unroll
  for (int s = 0; s < 2; ++s) {
    Zr = mf16(P[s], Cp[s], Zr); Zi = mf16(P[s], Sp[s], Zi);
    Zr = mf16(P[2 + s], Np[s], Zr); Zi = mf16(P[2 + s], Cp[s], Zi);
  }
  int em;
  auto finish = [&](auto SD) {
    constexpr int S = decltype(SD)::v, KO = 8 * S, SO = 8 - KO;
    u32x4 kr, ki, orr, oi;
    u32x4* e2w = (u32x4*)(smem + K2_E2 + w * 2048) + lane;
    const u32x4* e2r = (const u32x4*)(smem + K2_E2 + (w ^ 2) * 2048) + lane;
    int* ebx = (int*)(smem + K2_EB);
    if (lane == 0) ebx[w] = eb;
#pragma unroll
    for (int k = 0; k < 4; ++k) {
      const f32x2 dr = lo2(Zr, SO + 2 * k), di = lo2(Zi, SO + 2 * k), c = tc[4 * S + k], sn = ts[4 * S + k];
      orr[k] = pk_f16v(dr * c - di * sn); oi[k] = pk_f16v(di * c + dr * sn);
    }
    e2w[0] = orr; e2w[64] = oi;
#pragma unroll
    for (int k = 0; k < 4; ++k) {
      const f32x2 dr = lo2(Zr, KO + 2 * k), di = lo2(Zi, KO + 2 * k), c = tc[4 * S + k], sn = ts[4 * S + k];
      kr[k] = pk_f16v(dr * c - di * sn); ki[k] = pk_f16v(di * c + dr * sn);
    }
    wait_lgkm0();
    raw_bar();
    orr = e2r[0]; oi = e2r[64];
    const int eo = __builtin_amdgcn_readfirstlane(ebx[w ^ 2]);
    const int ebu = __builtin_amdgcn_readfirstlane(eb);
    em = max(ebu, eo);
    if (ebu != eo) {
      const u32 fk = pow2_h2(ebu - em), fo = pow2_h2(eo - em);
#pragma unroll
      for (int i = 0; i < 4; ++i) { kr[i] = pkmul_h(kr[i], fk); ki[i] = pkmul_h(ki[i], fk); orr[i] = pkmul_h(orr[i], fo); oi[i] = pkmul_h(oi[i], fo); }
    }
    Zr = zero16(); Zi = zero16();
    Zr = mf16(Cp[S], kr, Zr); Zi = mf16(Sp[S], kr, Zi);
    Zr = mf16(Np[S], ki, Zr); Zi = mf16(Cp[S], ki, Zi);
    Zr = mf16(Cp[1 - S], orr, Zr); Zi = mf16(Sp[1 - S], orr, Zi);
    Zr = mf16(Np[1 - S], oi, Zr); Zi = mf16(Cp[1 - S], oi, Zi);
  };
  if (sd) finish(IC<1>{}); else finish(IC<0>{});
  const bool prim = (sd == 1) == swp;
  const int ao = prim ? a1 : am, bo = prim ? b1 : bm;
  const u32 cj = sd ? 0x80000000u : 0u;
  if (valid && lane == 0) ecol[ao * 20 + bo] = (unsigned char)em;
  if (!direct) {
    u32x4* su = (u32x4*)(smem + K2_ST) + (((prim ? 0 : 2) + (bo & 1)) * 4) * 64 + lane;
#pragma unroll
    for (int g = 0; g < 4; ++g) {
      u32x4 o = {pk_f16(Zr[4 * g], Zi[4 * g]) ^ cj, pk_f16(Zr[4 * g + 1], Zi[4 * g + 1]) ^ cj,
                 pk_f16(Zr[4 * g + 2], Zi[4 * g + 2]) ^ cj, pk_f16(Zr[4 * g + 3], Zi[4 * g + 3]) ^ cj};
      su[64 * g] = o;
    }
    wait_lgkm0();
    raw_bar();
    const int c2 = b1 >> 1;
#pragma unroll
    for (int set = 0; set < 2; ++set) {
      const int duo = set ? (am * 10 + 9 - c2) : (a1 * 10 + c2);
      const u32x4* sp = (const u32x4*)(smem + K2_ST) + (set * 8 + w) * 64 + lane;
      const u32x4 p0 = sp[0], p1 = sp[256];
      u32x2* dp = (u32x2*)(X2 + (size_t)(duo >> 1) * 16384) + (duo & 1) + 2 * (32 * (8 * w + 4 * h) + r);
#pragma unroll
      for (int i = 0; i < 4; ++i) { u32x2 o = {p0[i], p1[i]}; dp[64 * i] = o; }
    }
  } else if (valid && !(self && sd)) {
    u32* du = (u32*)(X2 + (size_t)(bo >> 2) * 16384) + (bo & 3) + 4 * (128 * h + r);
#pragma unroll
    for (int i = 0; i < 16; ++i) du[128 * crow(i, 0)] = pk_f16(Zr[i], Zi[i]) ^ cj;
  }
}

constexpr int K2_W1_MAT = 28 * 1024 + 64;
DI void k2_w1(const u16* __restrict__ wg_, const u16* __restrict__ wu_, char* __restrict__ W1, char* smem, int idx) {
  const int lane = threadIdx.x & 63, w = wave_id();
  const int k16 = idx >> 1, th = idx & 1, mat = w >> 1;
  const char* src = (const char*)(mat ? wu_ : wg_);
  const u32 lds0 = lds_addr(smem);
#pragma unroll
  for (int n = 0; n < 14; ++n) {
    const int ins = (w & 1) * 14 + n;
    const int e = min(ins * 64 + lane, 1743), row = e / 109, c = e - 109 * row;
    const int d = k16 < 32 ? 20 * k16 + row : 80 * (k16 - 32) + 20 * (row >> 2) + 16 + (row & 3);
    dma<1>(min((u32)(d * 3456 + 1728 * th + 16 * c), 640u * 3456u - 16u), src, lds0 + mat * K2_W1_MAT + ins * 1024);
  }
  const int h2_ = lane >> 5, blk = (lane >> 4) & 1, q4 = (lane & 15) >> 2, p4 = lane & 3;
  const int r0 = (k16 < 32 ? 4 : 8) * h2_ + q4, dr = k16 < 32 ? 8 : 4;
  const char* lb = smem + blk * K2_W1_MAT + r0 * 1744 + 8 * p4;
  wait_vm<0>();
  raw_bar();
#pragma unroll
  for (int tq = 0; tq < 2; ++tq) {
    const int tl = 2 * w + tq;
    u32x4 o[7];
#pragma unroll
    for (int ft = 0; ft < 7; ++ft) {
      const u32x2 lo = tr_read(lb + 216 * tl + 32 * ft), hi = tr_read(lb + dr * 1744 + 216 * tl + 32 * ft);
      o[ft] = u32x4{lo[0], lo[1], hi[0], hi[1]};
    }
    if ((lane & 15) >= 12) o[6] = u32x4{0u, 0u, 0u, 0u};
    u32x4* dst = (u32x4*)(W1 + (size_t)(8 * th + tl) * (280 * 1024)) + lane;
#pragma unroll
    for (int ft = 0; ft < 7; ++ft) st16_wt(dst + (ft < 3 ? k16 * 3 + ft : 40 * ft + k16) * 64, o[ft]);
  }
}
static_assert(2 * K2_W1_MAT <= K2_LDS, "LDS");
__global__ __launch_bounds__(256, 2) void k2_kernel(const u16* __restrict__ wg_, const u16* __restrict__ wu_,
                                                    char* __restrict__ ws) {
  __shared__ __attribute__((aligned(16))) char smem[K2_LDS];
  const int bid = blockIdx.x;
  const int lw = bid < 168 ? (bid & 7) * 21 + (bid >> 3) : bid;
  if (lw < NCOLWG) k2_col(ws + X1_OFF, ws + WT_OFF, ws + X2_OFF, (unsigned char*)(ws + EC_OFF), smem, lw);
  else k2_w1(wg_, wu_, ws + W1_OFF, smem, lw - NCOLWG);
}

constexpr int R_BASE = 120 * 1024;
constexpr int R_X2 = R_BASE, R_EC = R_BASE + 10240, R_ST = R_BASE + 12288, R_H = R_X2, R_W2 = R_ST + 80 * 144;
static_assert(R_W2 + 12288 <= 160 * 1024, "LDS");
constexpr int PDN = 10, NWF = 12;
constexpr int bnd_slot(int c) { return 40 * c - PDN; }
constexpr int NEPI = 192;
constexpr int epi_slot(int u) { return u < 128 ? 121 + (u * 94) / 128 : 216 + ((u - 128) * 63) / 64; }
constexpr int epi_first(int n) { int u = 0; while (u < NEPI && epi_slot(u) < n) ++u; return u; }
constexpr int gst_slot(int e) { return epi_slot(32 * e + 31) + 1; }
constexpr int SIG_A = 240, POLL_A = 268;
constexpr int POLL_W = 160, CHECK_W = 178;
constexpr int SIG_B = 280 + 5 * 4, POLL_B = 280 + 5 * 15, CHECK_B = 280 + 5 * 20;
struct Ev { int slot, kind, a, b; };
constexpr int ev_size(const Ev& e) { return e.kind == 0 ? 4 : e.kind == 1 ? (e.b == 0 ? 4 : 3) : e.kind == 5 ? 5 : 1; }
template <int N> struct Sched {
  Ev ev[N];
  int n;
  constexpr void add(int slot, int kind, int a, int b) {
    int i = n++;
    while (i > 0 && ev[i - 1].slot > slot) { ev[i] = ev[i - 1]; --i; }
    ev[i] = Ev{slot, kind, a, b};
  }
  constexpr int first(int slot) const { int i = 0; while (i < n && ev[i].slot < slot) ++i; return i; }
  constexpr int before(int slot) const { int v = 0; for (int i = 0; i < n; ++i) v += ev[i].slot < slot ? ev_size(ev[i]) : 0; return v; }
  constexpr int end_of(int kind, int a, int b) const {
    int v = 0;
    for (int i = 0; i < n; ++i) { v += ev_size(ev[i]); if (ev[i].kind == kind && ev[i].a == a && ev[i].b == b) return v; }
    return -1000;
  }
};
constexpr Sched<48> make_s4() {
  Sched<48> s{};
  for (int g = 0; g < 10; ++g) s.add(g < 3 ? -1 : 12 * (g - 3) + 1, 0, g, 0);
  const int c0[7] = {80, 100, 124, 151, 181, 205, 232}, dp[7] = {6, 8, 9, 9, 8, 7, 6};
  for (int c = 0; c < 7; ++c) for (int p = 0; p < 3; ++p) s.add(c0[c] + dp[c] * p, 1, 3 + c, p);
  for (int e = 0; e < 6; ++e) s.add(gst_slot(e), 3, e, 0);
  s.add(POLL_A, 2, 0, 0); s.add(POLL_W, 2, 1, 0);
  return s;
}
constexpr Sched<76> make_s5() {
  Sched<76> s{};
  for (int k = 0; k < 56; ++k) s.add(k < 12 ? -2 : 280 + 5 * (k - 12) + 1, 4, k, 0);
  s.add(-1, 3, 6, 0);
  for (int c = 0; c < 4; ++c) for (int p = 0; p < 3; ++p) s.add(280 + 5 * (4 + 8 * c + 2 * p) + 3, 1, 10 + c, p);
  s.add(POLL_B, 2, 0, 0);
  s.add(280 + 5 * 45, 5, 0, 0);
  return s;
}
constexpr int cap63(int v) { return v < 63 ? v : 63; }
constexpr Sched<48> S4 = make_s4();
constexpr Sched<76> S5 = make_s5();

__global__ __launch_bounds__(256, 1) void k3_kernel(const u16* __restrict__ x, const u16* __restrict__ wd,
                                                    char* __restrict__ ws, u16* __restrict__ out) {
  __shared__ __attribute__((aligned(16))) char smem[160 * 1024];
  const int tid = threadIdx.x, lane = tid & 63, w = wave_id(), r = lane & 31, h = lane >> 5;
  const int bid = blockIdx.x, xcd = bid & 7, idx = bid >> 3;
  const int rb = 4 * (xcd & 3) + (idx & 3), t = 8 * (xcd >> 2) + (idx >> 2);
  const int mb = 2 * rb + (xcd >> 2), nb = idx >> 2;
  const u32 lds0 = lds_addr(smem), voff = lane * 16;
  const char* w1g = ws + W1_OFF + (size_t)t * (280 * 1024);
  const char* w2g = ws + W2_OFF + (size_t)nb * (280 * 1024);
  auto chunk_part = [&](auto C, auto P) {
    constexpr int c = decltype(C)::v, p = decltype(P)::v, o = p == 0 ? 0 : (p == 1 ? 4096 : 7168);
    const char* g = (c < 7 ? w1g + c * 40960 : w2g + (c - 7) * 40960) + w * 10240 + o;
    if constexpr (c < 7) dma<(p == 0 ? 4 : 3)>(voff, g, lds0 + (c & 3) * 40960 + w * 10240 + o);
    else dma_dev<(p == 0 ? 4 : 3)>(voff, g, lds0 + (c & 3) * 40960 + w * 10240 + o);
  };
  {
    const int wg = 16 * rb + t, m = 4 * wg + w;
    const char* X2 = ws + X2_OFF;
#pragma unroll
    for (int n = 0; n < 3; ++n) {
      const int pc = w + 4 * n;
      const u32 vx = (u32)(pc * 16 + (lane >> 2)) * 16384u + (u32)(wg * 64 + (lane & 3) * 16);
      const u32 ve = (u32)X2_SZ + (pc == 10 ? (u32)min(lane, 39) * 16u : 0u);
      dma<1>(pc < 10 ? vx : ve, X2, lds0 + R_X2 + pc * 1024);
    }
    u32x2 xr[6];
    {
      const char* src = (const char*)x + (size_t)m * 2560;
      const u32 vo = 40 * r + 8 * h, v2 = 40 * r + 32;
      asm volatile("global_load_dwordx2 %0, %6, %8\n\tglobal_load_dwordx2 %1, %6, %8 offset:16\n\tglobal_load_dwordx2 %2, %7, %8\n\t"
                   "global_load_dwordx2 %3, %6, %8 offset:1280\n\tglobal_load_dwordx2 %4, %6, %8 offset:1296\n\t"
                   "global_load_dwordx2 %5, %7, %8 offset:1280"
                   : "=&v"(xr[0]), "=&v"(xr[1]), "=&v"(xr[2]), "=&v"(xr[3]), "=&v"(xr[4]), "=&v"(xr[5])
                   : "v"(vo), "v"(v2), "s"(src) : "memory");
    }
#pragma unroll
    for (int n = 0; n < 3; ++n) {
      const int k32 = min(bid >> 2, 55), e = min((w + 4 * n) * 64 + lane, 639), row = e / 20, c = e - 20 * row, e2 = row >> 4;
      const int tt = k32 < 32 ? k32 >> 1 : (k32 < 48 ? k32 - 32 : 2 * (k32 - 48) + e2);
      const int ft = k32 < 32 ? 2 * (k32 & 1) + e2 : (k32 < 48 ? 4 + e2 : 6), fl = min(16 * ft + (row & 15), 107);
      dma<1>((u32)((108 * tt + fl) * 1280 + (bid & 3) * 320 + c * 16), wd, lds0 + R_W2 + (w + 4 * n) * 1024);
    }
    u32x4 BR[3], BI[3], Cp[2], Sp[2], Np[2];
    f32x2 tc[8], ts[8];
    {
      float c[8], s[8];
      dft32_consts(c, s, r, h);
      pack_csn(c, s, r, Cp, Sp, Np);
#pragma unroll
      for (int i = 0; i < 2; ++i) { pin(Cp[i]); pin(Sp[i]); pin(Np[i]); }
      const float rf = (float)r * (1.f / 640.f), r4 = rf * (float)(4 * h);
      static_for<0, 4>([&](auto Q) {
        constexpr int q = decltype(Q)::v;
#pragma unroll
        for (int i = 4 * q; i < 4 * q + 4; ++i) {
          const float rev = rf * (float)crow(i, 0) + r4;
          tc[i >> 1][i & 1] = cosr(rev); ts[i >> 1][i & 1] = sinr(rev);
          if constexpr (q < 3) {
            const float rev2 = rev * 32.f, c2 = cosr(rev2), s2 = sinr(rev2);
            const u32 br = pk_f16(c2, -s2), bi = pk_f16(s2, c2);
            BR[q][i & 3] = (i >= 8 && h) ? 0u : br; BI[q][i & 3] = (i >= 8 && h) ? 0u : bi;
          }
        }
        if constexpr (q < 3) { pin(BR[q]); pin(BI[q]); }
        pin(tc[2 * q]); pin(ts[2 * q]); pin(tc[2 * q + 1]); pin(ts[2 * q + 1]);
      });
    }
    wait_vm<6 + 3>();
    raw_bar();
    u32x4 a[3];
    u32 ec[3];
#pragma unroll
    for (int ks = 0; ks < 3; ++ks) {
      const int c = (ks == 2) ? 4 : 2 * ks + h;
      a[ks] = *(const u32x4*)(smem + R_X2 + (r * 5 + c) * 64 + w * 16);
      ec[ks] = *(const u32*)(smem + R_EC + (r * 5 + c) * 4);
    }
    wait_lgkm0();
    raw_bar();
    int ex[12];
    u32 em = 0;
#pragma unroll
    for (int i = 0; i < 12; ++i) { ex[i] = (ec[i >> 2] >> (8 * (i & 3))) & 0xff; em = umax(em, (u32)ex[i]); }
    const int E = (int)max64(em);
#pragma unroll
    for (int i = 0; i < 12; ++i) a[i >> 2][i & 3] = pkmul_h(a[i >> 2][i & 3], pow2_h2(ex[i] - (E + 5)));
    const float fs = __builtin_amdgcn_ldexpf(1.f / 1280.f, E - 127 - 9);
    f32x16 Tr, Ti;
    mfma_h<true>(Tr, a[0], BR[0]); mfma_h<true>(Ti, a[0], BI[0]);
    mfma_h<false>(Tr, a[1], BR[1]); mfma_h<false>(Ti, a[1], BI[1]);
    mfma_h<false>(Tr, a[2], BR[2]); mfma_h<false>(Ti, a[2], BI[2]);
    chunk_part(IC<0>{}, IC<0>{});
    mfma_done(Tr, Ti);
    u32x4 P[4];
#pragma unroll
    for (int k = 0; k < 8; ++k) {
      const f32x2 dr = lo2(Tr, 2 * k), di = lo2(Ti, 2 * k);
      P[k >> 2][k & 3] = pk_f16v(dr * tc[k] - di * ts[k]);
      P[2 + (k >> 2)][k & 3] = pk_f16v(di * tc[k] + dr * ts[k]);
    }
    pin(P[0]); pin(P[1]); pin(P[2]); pin(P[3]);
    chunk_part(IC<0>{}, IC<1>{});
    f32x16 Vr, Vi;
    mfma_h<true>(Vr, P[0], Cp[0]); mfma_h<true>(Vi, P[0], Sp[0]);
    mfma_h<false>(Vr, P[2], Np[0]); mfma_h<false>(Vi, P[2], Cp[0]);
    mfma_h<false>(Vr, P[1], Cp[1]); mfma_h<false>(Vi, P[1], Sp[1]);
    mfma_h<false>(Vr, P[3], Np[1]); mfma_h<false>(Vi, P[3], Cp[1]);
    chunk_part(IC<0>{}, IC<2>{});
    mfma_done(Vr, Vi);
    asm volatile("s_waitcnt vmcnt(13)" : "+v"(xr[0]), "+v"(xr[1]), "+v"(xr[2]), "+v"(xr[3]), "+v"(xr[4]), "+v"(xr[5])::"memory");
    f32x2 hv[2][6];
    u32 hp[2][6];
    float mean[2], var[2];
#pragma unroll
    for (int rr = 0; rr < 2; ++rr) {
      f32x2 sm = {0.f, 0.f};
#pragma unroll
      for (int k = 0; k < 6; ++k) {
        const f32x2 y = (rr ? lo2(Vi, 2 * k) : lo2(Vr, 2 * k)) * fs;
        const u32 yb = pk_bf16(y[0], y[1]);
        const u32 xx = xr[3 * rr + (k >> 1)][k & 1];
        const f32x2 hs = f32x2{bf_lo(xx), bf_hi(xx)} + f32x2{bf_lo(yb), bf_hi(yb)};
        u32 hh = pk_bf16(hs[0], hs[1]);
        if (k >= 4) hh = h ? 0u : hh;
        hp[rr][k] = hh;
        hv[rr][k] = f32x2{bf_lo(hh), bf_hi(hh)};
        sm += hv[rr][k];
      }
      mean[rr] = sm[0] + sm[1];
    }
    pin(mean[0]); pin(mean[1]);
    chunk_part(IC<1>{}, IC<0>{});
#pragma unroll
    for (int rr = 0; rr < 2; ++rr) {
      char* hl = smem + R_H + (2 * w + rr) * 1280 + 40 * r;
      *(u32x2*)(hl + 8 * h) = u32x2{hp[rr][0], hp[rr][1]};
      *(u32x2*)(hl + 8 * h + 16) = u32x2{hp[rr][2], hp[rr][3]};
      if (h == 0) *(u32x2*)(hl + 32) = u32x2{hp[rr][4], hp[rr][5]};
    }
#pragma unroll
    for (int rr = 0; rr < 2; ++rr) mean[rr] = sum64(mean[rr]) * (1.f / 640.f);
    pin(mean[0]); pin(mean[1]);
    chunk_part(IC<1>{}, IC<1>{});
#pragma unroll
    for (int rr = 0; rr < 2; ++rr) {
      f32x2 q2 = {0.f, 0.f}, q3 = {0.f, 0.f};
#pragma unroll
      for (int k = 0; k < 4; ++k) { const f32x2 d = hv[rr][k] - mean[rr]; q2 += d * d; }
#pragma unroll
      for (int k = 4; k < 6; ++k) { const f32x2 d = hv[rr][k] - mean[rr]; q3 += d * d; }
      var[rr] = q2[0] + q2[1] + (h ? 0.f : q3[0] + q3[1]);
    }
    pin(var[0]); pin(var[1]);
    chunk_part(IC<1>{}, IC<2>{});
#pragma unroll
    for (int rr = 0; rr < 2; ++rr) var[rr] = sum64(var[rr]) * (1.f / 640.f);
#pragma unroll
    for (int rr = 0; rr < 2; ++rr) {
      float mbn, inv;
      ln_scalars(mean[rr], var[rr], mbn, inv);
      u32 o[6];
#pragma unroll
      for (int k = 0; k < 6; ++k) {
        const f32x2 d0 = hv[rr][k] - mbn;
        const u32 d = pk_bf16(d0[0], d0[1]);
        const f32x2 n0 = f32x2{bf_lo(d), bf_hi(d)} * inv;
        o[k] = pk_bf16(n0[0], n0[1]);
      }
      const int rowl = 2 * w + rr;
      u32x4 o4 = {o[0], o[1], o[2], o[3]};
      *(u32x4*)(smem + R_ST + (h * 32 + r) * 144 + rowl * 16) = o4;
      if (h == 0) { u32x2 o2 = {o[4], o[5]}; *(u32x2*)(smem + R_ST + (64 + (r >> 1)) * 144 + rowl * 16 + (r & 1) * 8) = o2; }
    }
    wait_lgkm0();
    raw_bar();
    const int row0 = 8 * wg;
    const char* ab = ws + ACT_OFF + (size_t)(row0 >> 5) * (40 * 1024) + (row0 & 31) * 16;
#pragma unroll
    for (int it = 0; it < 3; ++it) {
      const int gi = (tid >> 3) + 32 * it;
      if (gi < 80) {
        const int L = gi < 64 ? (gi & 1) * 32 + (gi >> 1) : gi;
        st16_dev(gi * 512 + (tid & 7) * 16, ab, *(const u32x4*)(smem + R_ST + L * 144 + (tid & 7) * 16));
      }
    }
  }
  auto pre1 = [&] {
    const char* hb = ws + H_OFF + (size_t)(16 * rb + t) * 10240;
#pragma unroll
    for (int i = 0; i < 4; ++i) {
      const int c = tid - 64 + 192 * i;
      if (c < 640) st16_dev(c * 16, hb, *(const u32x4*)(smem + R_H + c * 16));
    }
    if (bid < 224) {
      const int g4 = lane >> 4, k32 = bid >> 2;
      const char* lb2 = smem + R_W2 + (16 * (g4 >> 1) + 4 * (g4 & 1) + ((lane & 15) >> 2)) * 320 + (lane & 3) * 8;
#pragma unroll
      for (int i = 0; i < 4; ++i) {
        const int fr = min(w - 1 + 3 * i, 9), nbl = fr / 5;
        u32x2 lo = tr_read(lb2 + 32 * fr), hi = tr_read(lb2 + 32 * fr + 8 * 320);
        if (k32 >= 48 && (g4 & 1)) hi = u32x2{0u, 0u};
        st16_dev(voff, ws + W2_OFF + ((size_t)((2 * (bid & 3) + nbl) * 56 + k32) * 5 + (fr - 5 * nbl)) * 1024,
                 u32x4{lo[0], lo[1], hi[0], hi[1]});
      }
    }
#pragma unroll
    for (int g = 0; g < 4; ++g)
      if (3 * g + w - 1 < 10) dma<4>(voff, w1g + 81920 + (3 * g + w - 1) * 4096, lds0 + 81920 + (3 * g + w - 1) * 4096);
  };
  publish_and_wait((u32*)(ws + CNT_OFF + rb * 256), tid, w, pre1);

  u32 lb[3];
#pragma unroll
  for (int i = 0; i < 3; ++i) { lb[i] = lane * 16 + i * 65536; pin(lb[i]); }
  u32x4 wf[NWF];
  u32 pollv;
  u32* cntA = (u32*)(ws + CNT_OFF + (16 + rb) * 256);
  u32* cntB = (u32*)(ws + CNT_OFF + (32 + rb) * 256);
  u32* cntW = (u32*)(ws + CNT_OFF + (48 + (nb >> 1)) * 256);
  auto rdw = [&](auto N) {
    constexpr int n = decltype(N)::v, o = (n % 160) * 1024;
    if constexpr (n < 560) wf[n % NWF] = *(const u32x4*)(smem + lb[o >> 16] + (o & 65535));
  };
  auto boundary = [&](auto WN) {
    wait_lgkm0();
    wait_vm<decltype(WN)::v>();
    raw_bar();
  };
  u32x4 gf[56];
  const char* gg = ws + G_OFF + (size_t)(mb * 56) * 4096 + w * 1024;
  {
    u32x4 act[40];
    const char* actg = ws + ACT_OFF + (size_t)(4 * rb + w) * (40 * 1024);
    f32x16 acc[7];
    u32x4 gout;
    float ez[2][2];
    const int m = 128 * rb + 32 * w + r;
    const char* gdst = ws + G_OFF + (size_t)((2 * rb + (w >> 1)) * 56) * 4096 + (w & 1) * 2048;
    const u32 gvo = (r >> 4) * 1024 + (r & 15) * 16 + h * 256;
    float k1 = -2.3022081983f;
    pin(k1);
    auto epi = [&](auto U) {
      constexpr int u = decltype(U)::v, z = u & 1, st = (u >> 1) & 7, pr = 2 * (u >> 4) + z, ft = pr / 4, p = pr % 4;
#pragma unroll
      for (int e = 0; e < 2; ++e) {
        const float g = acc[ft][2 * p + e];
        if constexpr (st == 0) ez[z][e] = vmul(g, g);
        if constexpr (st == 1) ez[z][e] = vpoly(ez[z][e], k1);
        if constexpr (st == 2) ez[z][e] = vmul(ez[z][e], g);
        if constexpr (st == 3) ez[z][e] = vexp(ez[z][e]);
        if constexpr (st == 4) { ez[z][e] = vadd1(ez[z][e]); }
        if constexpr (st == 5) ez[z][e] = vrcp(ez[z][e]);
        if constexpr (st == 6) ez[z][e] = vmul(g, ez[z][e]);
        if constexpr (st == 7) ez[z][e] = vmul(ez[z][e], acc[ft][8 + 2 * p + e]);
      }
      if constexpr (st == 7) gout[p] = pk_bf16(ez[z][0], ez[z][1]);
    };
    auto gstore = [&](int ft) {
      const int kk = ft < 4 ? 2 * t + (ft >> 1) : (ft < 6 ? 32 + t : 48 + (t >> 1)), g2 = ft < 6 ? ft & 1 : t & 1;
      st16_dev(gvo, gdst + kk * 4096 + g2 * 512, gout);
    };
    auto events = [&](auto N) {
      static_for<S4.first(decltype(N)::v), S4.first(decltype(N)::v + 1)>([&](auto I) {
        constexpr Ev e = S4.ev[decltype(I)::v];
        {
          if constexpr (e.kind == 0) ld4_dev(act[4 * e.a], act[4 * e.a + 1], act[4 * e.a + 2], act[4 * e.a + 3], voff, actg + e.a * 4096);
          if constexpr (e.kind == 1) chunk_part(IC<e.a>{}, IC<e.b>{});
          if constexpr (e.kind == 3) gstore(e.a);
          if constexpr (e.kind == 2) poll_issue(pollv, e.a ? cntW : cntA);
        }
      });
    };
    events(IC<-1>{});
    static_for<0, PDN>(rdw);
    static_for<0, 280>([&](auto N) {
      constexpr int n = decltype(N)::v;
      constexpr int ft = n < 120 ? n % 3 : n / 40, k16 = n < 120 ? n / 3 : n % 40;
      if constexpr (n < 120 && n % 6 == 0)
        wait_a2<S4.before(n) - (S4.end_of(0, k16 / 4, 0) - 2 + k16 % 4)>(act[k16], act[k16 + 1]);
      mfma32<k16 == 0>(acc[ft], wf[n % NWF], act[k16]);
      __builtin_amdgcn_sched_barrier(0);
      if constexpr ((n + PDN) % 40 == 0 && n + PDN < 280) {
        constexpr int c = (n + PDN) / 40;
        boundary(IC<cap63(c < 2 ? 63 : c == 2 ? S4.before(n) : S4.before(n) - S4.end_of(1, c, 2))>{});
      }
      if constexpr (n == bnd_slot(1)) { if (bid < 224) signal((u32*)(ws + CNT_OFF + (48 + (bid & 3)) * 256), tid); }
      if constexpr (n == CHECK_W) poll_check<S4.before(n) - S4.end_of(2, 1, 0)>(pollv, cntW, 56u);
      if constexpr (n == SIG_A) {
        wait_vm<S4.before(n) - S4.end_of(3, 3, 0)>();
        raw_bar();
        signal(cntA, tid);
      }
      if constexpr (n + PDN < 280) rdw(IC<n + PDN>{});
      static_for<0, 2>([&](auto K) {
        constexpr int u = epi_first(n) + decltype(K)::v;
        if constexpr (u < NEPI) { if constexpr (epi_slot(u) == n) epi(IC<u>{}); }
      });
      events(N);
      __builtin_amdgcn_sched_barrier(0);
    });
    poll_check<S4.before(1000) - S4.end_of(2, 0, 0)>(pollv, cntA);
    static_for<0, 12>([&](auto K) { ld1_dev(gf[decltype(K)::v], voff, gg + decltype(K)::v * 4096); });
    asm volatile("s_nop 7\n\ts_nop 7" : "+v"(acc[6]));
#pragma unroll
    for (int p = 0; p < 4; ++p) {
      const f32x2 g = lo2(acc[6], 2 * p), u = lo2(acc[6], 8 + 2 * p);
      const f32x2 z = g * (g * g * -0.1029432397f + -2.3022081983f);
      const f32x2 den = f32x2{__builtin_amdgcn_exp2f(z[0]), __builtin_amdgcn_exp2f(z[1])} + 1.f;
      const f32x2 ov = (g * f32x2{__builtin_amdgcn_rcpf(den[0]), __builtin_amdgcn_rcpf(den[1])}) * u;
      gout[p] = pk_bf16(ov[0], ov[1]);
    }
    gstore(6);
  }
  boundary(IC<S4.before(1000) + 13 - S4.end_of(1, 7, 2)>{});
  {
    u32x2 hv[5];
    const int mrow = 64 * mb + 16 * w + (lane & 15), ncol = 80 * nb + 4 * (lane >> 4);
    auto events = [&](auto N) {
      static_for<S5.first(decltype(N)::v), S5.first(decltype(N)::v + 1)>([&](auto I) {
        constexpr Ev e = S5.ev[decltype(I)::v];
        {
          if constexpr (e.kind == 4) ld1_dev(gf[e.a], voff, gg + e.a * 4096);
          if constexpr (e.kind == 1) chunk_part(IC<e.a>{}, IC<e.b>{});
          if constexpr (e.kind == 2) poll_issue(pollv, cntB);
          if constexpr (e.kind == 5) ldh_dev(hv, (u32)(mrow * 640 + ncol) * 2u, ws + H_OFF);
        }
      });
    };
    f32x4 acc[5];
    static_for<280, 280 + PDN>(rdw);
    static_for<280, 560>([&](auto N) {
      constexpr int n = decltype(N)::v, k = (n - 280) / 5, nt = (n - 280) % 5;
      if constexpr (nt == 0 && k % 2 == 0) wait_a2<S5.before(n) - S5.end_of(4, k + 1, 0)>(gf[k], gf[k + 1]);
      mfma16<k == 0>(acc[nt], wf[n % NWF], gf[k]);
      __builtin_amdgcn_sched_barrier(0);
      if constexpr ((n + PDN) % 40 == 0 && n + PDN < 560) {
        constexpr int c = (n + PDN) / 40;
        boundary(IC<cap63(c < 10 ? 63 : S5.before(n) - S5.end_of(1, c, 2))>{});
      }
      if constexpr (n == SIG_B) {
        wait_vm<S5.before(n) - S5.end_of(3, 6, 0)>();
        raw_bar();
        signal(cntB, tid);
      }
      if constexpr (n == CHECK_B) poll_check<S5.before(n) - S5.end_of(2, 0, 0)>(pollv, cntB);
      rdw(IC<n + PDN>{});
      events(N);
      __builtin_amdgcn_sched_barrier(0);
    });
    asm volatile("s_waitcnt vmcnt(0)\n\ts_nop 7\n\ts_nop 7"
                 : "+v"(hv[0]), "+v"(hv[1]), "+v"(hv[2]), "+v"(hv[3]), "+v"(hv[4]), "+v"(acc[0]), "+v"(acc[1]), "+v"(acc[2]),
                   "+v"(acc[3]), "+v"(acc[4])::"memory");
    u16* op = out + (size_t)mrow * 640 + ncol;
#pragma unroll
    for (int j = 0; j < 5; ++j) {
      const u32 o0 = pk_bf16(acc[j][0], acc[j][1]), o1 = pk_bf16(acc[j][2], acc[j][3]);
      u32x2 o = {pk_bf16(bf_lo(hv[j][0]) + bf_lo(o0), bf_hi(hv[j][0]) + bf_hi(o0)),
                 pk_bf16(bf_lo(hv[j][1]) + bf_lo(o1), bf_hi(hv[j][1]) + bf_hi(o1))};
      *(u32x2*)(op + 16 * j) = o;
    }
  }
}

extern "C" void kernel_launch(void* const* d_in, const int* in_sizes, int n_in, void* d_out, int out_size, void* d_ws,
                              size_t ws_size, hipStream_t stream) {
  const u16* x = (const u16*)d_in[0];
  const u16* wr = (const u16*)d_in[1];
  const u16* wi = (const u16*)d_in[2];
  const u16* wg = (const u16*)d_in[4];
  const u16* wu = (const u16*)d_in[5];
  const u16* wd = (const u16*)d_in[6];
  char* ws = (char*)d_ws;
  k1_kernel<<<K1_GRID + K1_WARM, 256, 0, stream>>>(x, wr, wi, ws);
  k2_kernel<<<K2_GRID, 256, 0, stream>>>(wg, wu, ws);
  k3_kernel<<<256, 256, 0, stream>>>(x, wd, ws, (u16*)d_out);
}
```
